```python
import torch

def _softmax(x, axis=-1):
    return torch.softmax(x, dim=axis)
def _gelu(x):
    return torch.nn.functional.gelu(x, approximate="tanh")
def _silu(x):
    return torch.nn.functional.silu(x)
def _sigmoid(x):
    return torch.sigmoid(x)
def _layernorm(x, axis=-1, eps=1e-5):
    m = x.mean(dim=axis, keepdim=True)
    v = x.var(dim=axis, keepdim=True, unbiased=False)
    return (x - m) / torch.sqrt(v + eps)
def _scatter_add(tgt, idx, src, axis=0):
    return tgt.clone().index_add_(axis, idx, src)
def _segment_sum(src, seg, n):
    out = torch.zeros((n,) + src.shape[1:], dtype=src.dtype)
    return out.index_add_(0, seg, src)
def _pad(x, pw):
    flat = tuple(v for pair in reversed(pw) for v in pair)
    return torch.nn.functional.pad(x, flat)

kernel_name = 'gated_mix__sliding_block_cached__glu_gated_swiglu__decode'

def setup_inputs():

    _w = (lambda a: (lambda t: t.to(torch.bfloat16) if t.is_floating_point() else t)(torch.as_tensor(a)))
    def _A_setup():
        B, Sq, D = 4, 1, 512
        Hq, Hkv, Sk, Dh = 32, 8, 8192, 128
        assert Hq % Hkv == 0, (Hq, Hkv)
        return {
            "x": _w((lambda *s: torch.randn(*s, dtype=torch.bfloat16) * 0.05)(B, Sq, D)),
            "k_cache": _w((lambda *s: torch.randn(*s, dtype=torch.bfloat16) * 6.4)(B, Hkv, Sk, Dh)),
            "v_cache": _w((lambda *s: torch.randn(*s, dtype=torch.bfloat16) * 0.05)(B, Hkv, Sk, Dh)),
            "wq": _w((lambda *s: torch.randn(*s, dtype=torch.bfloat16) * 6.4)(D, Hq * Dh) * (D ** -0.5)),
            "wo": _w((lambda *s: torch.randn(*s, dtype=torch.bfloat16) * 0.05)(Hq * Dh, D) * ((Hq * Dh) ** -0.5)),
        }
    _A_in = _A_setup()

    def _B_setup():
        B, S, D, F = 4, 1, 512, 1792
        return {
            "x": _w((lambda *s: torch.randn(*s, dtype=torch.bfloat16) * 0.05)(B, S, D)),
            "w1": _w((lambda *s: torch.randn(*s, dtype=torch.bfloat16) * 0.4)(D, F) * (2.0 / D) ** 0.5),
            "w3": _w((lambda *s: torch.randn(*s, dtype=torch.bfloat16) * 0.4)(D, F) * (2.0 / D) ** 0.5),
            "w2": _w((lambda *s: torch.randn(*s, dtype=torch.bfloat16) * 0.4)(F, D) * (2.0 / F) ** 0.5),
        }
    _B_in = _B_setup()

    return {
        "A_x": _A_in["x"],
        "A_k_cache": _A_in["k_cache"],
        "A_v_cache": _A_in["v_cache"],
        "A_wq": _A_in["wq"],
        "A_wo": _A_in["wo"],
        "B_x": _B_in["x"],
        "B_w1": _B_in["w1"],
        "B_w3": _B_in["w3"],
        "B_w2": _B_in["w2"],
        "gate_w": _w((lambda *s: torch.randn(*s, dtype=torch.bfloat16) * 0.05)(512) * 16.0),
    }

def reference(A_x, A_k_cache, A_v_cache, A_wq, A_wo, B_x, B_w1, B_w3, B_w2, gate_w):
    def _A_ref(x, k_cache, v_cache, wq, wo):
        B, Sq, D = x.shape
        Hq, Dh = 32, 128
        Hkv, Sk = k_cache.shape[1], k_cache.shape[2]
        W = min(4096, max(256, Sk // 4))
        rep = Hq // Hkv
        q = (lambda x, *d: x.permute(*d))((x @ wq).reshape(B, Sq, Hq, Dh), 0, 2, 1, 3)
        k = (lambda x, n, axis: torch.repeat_interleave(x, n, dim=axis))(k_cache[:, :, Sk - W:, :], rep, axis=1)
        v = (lambda x, n, axis: torch.repeat_interleave(x, n, dim=axis))(v_cache[:, :, Sk - W:, :], rep, axis=1)
        scores = torch.einsum("bhqd,bhkd->bhqk", q, k) * (Dh ** -0.5)
        p = _softmax(scores, axis=-1)
        o = torch.einsum("bhqk,bhkd->bhqd", p, v)
        return (lambda x, *d: x.permute(*d))(o, 0, 2, 1, 3).reshape(B, Sq, Hq * Dh) @ wo

    def _B_ref(x, w1, w3, w2):
        gate = torch.einsum("bsd,df->bsf", x, w1)
        up = torch.einsum("bsd,df->bsf", x, w3)
        h = _silu(gate) * up
        out = torch.einsum("bsf,fd->bsd", h, w2)
        return out

    x = A_x
    g = _sigmoid((x * gate_w).sum(-1, keepdims=True))
    a = _A_ref(x, A_k_cache, A_v_cache, A_wq, A_wo)
    b = _B_ref(x, B_w1, B_w3, B_w2)
    return g * a + (1 - g) * b
```

```cpp
#include <hip/hip_runtime.h>
#include <hip/hip_bf16.h>
#include <stdint.h>

typedef unsigned short u16;
typedef unsigned u32x4 __attribute__((ext_vector_type(4)));

__device__ __forceinline__ float bf2f(u16 b) { return __uint_as_float((unsigned)b << 16); }
__device__ __forceinline__ float blo(unsigned w) { return __uint_as_float(w << 16); }
__device__ __forceinline__ float bhi(unsigned w) { return __uint_as_float(w & 0xffff0000u); }
__device__ __forceinline__ float rbf(float f) { return (float)(__bf16)f; }

constexpr int B = 4, D = 512, HQ = 32, HKV = 8, SK = 8192, DH = 128, W = 2048, F = 1792, NS = 8;

__global__ void __launch_bounds__(64) k1(const u16* x, const u16* wq, const u16* w1, const u16* w3, float* q, float* h) {
  __shared__ float xs[B * D];
  for (int i = threadIdx.x; i < B * D; i += 64) xs[i] = bf2f(x[i]);
  __syncthreads();
  int c = blockIdx.x * 64 + threadIdx.x;
  if (c < HQ * DH) {
    float a[B] = {0, 0, 0, 0};
    for (int k = 0; k < D; ++k) { float w = bf2f(wq[(size_t)k * (HQ * DH) + c]); for (int b = 0; b < B; ++b) a[b] += xs[b * D + k] * w; }
    for (int b = 0; b < B; ++b) q[b * HQ * DH + c] = rbf(a[b]);
  } else {
    c -= HQ * DH;
    float a[B] = {0, 0, 0, 0}, u[B] = {0, 0, 0, 0};
    for (int k = 0; k < D; ++k) { float w = bf2f(w1[(size_t)k * F + c]), v = bf2f(w3[(size_t)k * F + c]);
      for (int b = 0; b < B; ++b) { a[b] += xs[b * D + k] * w; u[b] += xs[b * D + k] * v; } }
    for (int b = 0; b < B; ++b) { float g = a[b]; float s = g / (1.f + __expf(-g)); h[b * F + c] = s * u[b]; }
  }
}

__global__ void __launch_bounds__(256) k2(const u16* kc, const u16* vc, const float* q, float* po, float* pml) {
  __shared__ float qs[4 * DH];
  __shared__ float ps[4][256];
  __shared__ float red[4][4];
  const int s = blockIdx.x, kvh = blockIdx.y, b = blockIdx.z, t = threadIdx.x;
  for (int i = t; i < 4 * DH; i += 256) qs[i] = q[b * HQ * DH + kvh * 4 * DH + i];
  __syncthreads();
  const size_t base = ((size_t)(b * HKV + kvh) * SK + (SK - W) + s * 256) * DH;
  const u32x4* kr = (const u32x4*)(kc + base + (size_t)t * DH);
  float sc[4] = {0, 0, 0, 0};
  for (int c = 0; c < 16; ++c) { u32x4 w = kr[c];
    for (int j = 0; j < 4; ++j) { float l = blo(w[j]), hh = bhi(w[j]);
      for (int g = 0; g < 4; ++g) sc[g] += l * qs[g * DH + c * 8 + 2 * j] + hh * qs[g * DH + c * 8 + 2 * j + 1]; } }
  float m[4];
  for (int g = 0; g < 4; ++g) { sc[g] = rbf(rbf(sc[g]) * 0.08838834764831845f); float v = sc[g];
    for (int o = 32; o; o >>= 1) v = fmaxf(v, __shfl_xor(v, o)); if ((t & 63) == 0) red[g][t >> 6] = v; }
  __syncthreads();
  for (int g = 0; g < 4; ++g) m[g] = fmaxf(fmaxf(red[g][0], red[g][1]), fmaxf(red[g][2], red[g][3]));
  __syncthreads();
  for (int g = 0; g < 4; ++g) { float p = __expf(sc[g] - m[g]); ps[g][t] = p; float v = p;
    for (int o = 32; o; o >>= 1) v += __shfl_xor(v, o); if ((t & 63) == 0) red[g][t >> 6] = v; }
  __syncthreads();
  const int d = t & 127, gh = (t >> 7) * 2;
  float o0 = 0, o1 = 0;
  for (int k = 0; k < 256; ++k) { float v = bf2f(vc[base + (size_t)k * DH + d]); o0 += ps[gh][k] * v; o1 += ps[gh + 1][k] * v; }
  size_t row0 = ((size_t)(b * HQ + kvh * 4 + gh) * NS + s);
  po[row0 * DH + d] = o0; po[(row0 + NS) * DH + d] = o1;
  if (t < 4) { size_t r = ((size_t)(b * HQ + kvh * 4 + t) * NS + s); pml[2 * r] = m[t]; pml[2 * r + 1] = red[t][0] + red[t][1] + red[t][2] + red[t][3]; }
}

__global__ void __launch_bounds__(256) k3(const u16* x, const u16* gw, const u16* wo, const u16* w2, const float* po, const float* pml, const float* h, u16* out) {
  __shared__ float os[HQ * DH];
  __shared__ float hs[F];
  __shared__ float red[2][4][64];
  __shared__ float gred[4];
  const int b = blockIdx.y, t = threadIdx.x;
  for (int i = t; i < HQ * DH; i += 256) { int hd = i >> 7, d = i & 127; size_t r = (size_t)(b * HQ + hd) * NS;
    float M = -INFINITY; for (int s = 0; s < NS; ++s) M = fmaxf(M, pml[2 * (r + s)]);
    float L = 0, o = 0; for (int s = 0; s < NS; ++s) { float w = __expf(pml[2 * (r + s)] - M); L += w * pml[2 * (r + s) + 1]; o += w * po[(r + s) * DH + d]; }
    os[i] = o / L; }
  for (int i = t; i < F; i += 256) hs[i] = h[b * F + i];
  float gs = 0; for (int i = t; i < D; i += 256) gs += bf2f(x[b * D + i]) * bf2f(gw[i]);
  for (int o = 32; o; o >>= 1) gs += __shfl_xor(gs, o);
  if ((t & 63) == 0) gred[t >> 6] = gs;
  __syncthreads();
  const int c = blockIdx.x * 64 + (t & 63), kq = t >> 6;
  float a = 0, bb = 0;
  for (int k = kq * 1024; k < kq * 1024 + 1024; ++k) a += os[k] * bf2f(wo[(size_t)k * D + c]);
  for (int k = kq * 448; k < kq * 448 + 448; ++k) bb += hs[k] * bf2f(w2[(size_t)k * D + c]);
  red[0][kq][t & 63] = a; red[1][kq][t & 63] = bb;
  __syncthreads();
  if (t < 64) { float A = red[0][0][t] + red[0][1][t] + red[0][2][t] + red[0][3][t], Bv = red[1][0][t] + red[1][1][t] + red[1][2][t] + red[1][3][t];
    float g = 1.f / (1.f + __expf(-(gred[0] + gred[1] + gred[2] + gred[3])));
    __bf16 r = (__bf16)(g * A + (1.f - g) * Bv); out[b * D + c] = __builtin_bit_cast(u16, r); }
}

extern "C" void kernel_launch(void* const* d_in, const int* in_sizes, int n_in, void* d_out, int out_size, void* d_ws, size_t ws_size, hipStream_t stream) {
  const u16* x = (const u16*)d_in[0]; const u16* kc = (const u16*)d_in[1]; const u16* vc = (const u16*)d_in[2];
  const u16* wq = (const u16*)d_in[3]; const u16* wo = (const u16*)d_in[4];
  const u16* w1 = (const u16*)d_in[6]; const u16* w3 = (const u16*)d_in[7]; const u16* w2 = (const u16*)d_in[8]; const u16* gw = (const u16*)d_in[9];
  float* q = (float*)d_ws; float* h = q + B * HQ * DH; float* po = h + B * F; float* pml = po + B * HQ * NS * DH;
  k1<<<(HQ * DH + F) / 64, 64, 0, stream>>>(x, wq, w1, w3, q, h);
  k2<<<dim3(NS, HKV, B), 256, 0, stream>>>(kc, vc, q, po, pml);
  k3<<<dim3(D / 64, B), 256, 0, stream>>>(x, gw, wo, w2, po, pml, h, (u16*)d_out);
}
```

```cpp
#include <hip/hip_runtime.h>
#include <stdint.h>

typedef unsigned short u16;
typedef unsigned u32;
typedef unsigned long long u64;
typedef u32 u32x4 __attribute__((ext_vector_type(4)));
typedef u32 u32x2 __attribute__((ext_vector_type(2)));
typedef float f32x4 __attribute__((ext_vector_type(4)));
typedef float f32x2 __attribute__((ext_vector_type(2)));
typedef short s16x4 __attribute__((ext_vector_type(4)));
typedef __bf16 bf16x8 __attribute__((ext_vector_type(8)));
typedef __bf16 bf16x2 __attribute__((ext_vector_type(2)));
#define LAS __attribute__((address_space(3)))
#define GAS __attribute__((address_space(1)))
typedef __amdgpu_buffer_rsrc_t rsrc_t;

constexpr int SK = 8192, WIN = 2048;
constexpr int PACE = 56;
constexpr int P0 = 80;
constexpr int G2T = P0 + 8 * PACE + 20, G2S = 3 * PACE / 8;
constexpr int T1 = 300, DQ = 90;
constexpr int RG = 266496;
constexpr int WS_QB = 4096, WS_FB = WS_QB + 32768, WS_MB = WS_FB + 32768, WS_C2 = WS_MB + 2048, WS_C3 = WS_C2 + 2048, WS_PB = WS_C3 + 2048,
              WS_OB = WS_PB + 65536, WS_END = 8 * RG;
static_assert(WS_OB + 65536 <= RG, "region map");
constexpr int L_PO = 0, L_P = 8192, L_X = L_P + 2048, XS = 1040, L_Q = L_X + 4 * XS, QS = 272, L_M = L_Q + 4 * QS, L_L = L_M + 64,
              L_O = L_L + 64, OS = 144, L_S = L_O + 32 * OS, L_H = L_S + 128, HS = 80, L_G = L_H + 4 * HS, L_CNT = L_G + 16,
              L_G2 = (L_CNT + 64 + 255) & ~255, L_G1 = L_G2 + 24576, L_K = L_G1 + 32768, L_END = L_K + 65536;
constexpr int C_G1 = L_CNT, C_Q = L_CNT + 4, C_M = L_CNT + 8, C_V = L_CNT + 12, C_G2 = L_CNT + 16, C_GO = L_CNT + 20, C_F1 = L_CNT + 24,
              C_T0 = L_CNT + 28, C_P = L_CNT + 32;
constexpr int AUX_NT = 2, AUX_SC1 = 16;
constexpr int SPIN_MAX = 1 << 20, HINT_MAX = 512;

#define PIN(...) asm volatile("" : __VA_ARGS__)

__device__ __forceinline__ int c3_at(int j) { return (j & 7) * RG + WS_C3 + (j >> 3) * 256; }
__device__ __forceinline__ void gate(u32 until) {
  asm volatile("1:\n\ts_memrealtime vcc\n\ts_waitcnt lgkmcnt(0)\n\ts_sub_i32 vcc_lo, vcc_lo, %0\n\ts_cmp_ge_i32 vcc_lo, 0\n\ts_cbranch_scc1 2f\n\ts_sleep 1\n\ts_branch 1b\n2:"
               :: "s"(until) : "memory", "vcc", "scc");
}
__device__ __forceinline__ rsrc_t mk(const void* p, u32 n) { return __builtin_amdgcn_make_buffer_rsrc((void*)p, 0, n, 0x00020000); }
template <int AUX> __device__ __forceinline__ u32x4 ld(rsrc_t r, int off) { return __builtin_amdgcn_raw_buffer_load_b128(r, off, 0, AUX); }
__device__ __forceinline__ void st16(rsrc_t r, int off, u32x4 v) { __builtin_amdgcn_raw_buffer_store_b128(v, r, off, 0, AUX_SC1); }
#define PISSUE "global_atomic_add_x2 v[160:161], %[p], %[z], off sc0 sc1\n\t"
#define PCHECK "s_sleep 1\n\tv_cmp_ne_u64 vcc, v[160:161], %[want]\n\ts_cbranch_vccz 2f\n\t"
template <int MASK> __device__ __forceinline__ void poll_first(GAS u64* p) {
  u64 sv; u64 zero = 0;
  asm volatile("s_mov_b64 %[sv], exec\n\ts_mov_b64 exec, %[mask]\n\tv_mov_b64 v[160:161], -1\n\t" PISSUE "s_mov_b64 exec, %[sv]"
               : [sv] "=&s"(sv) : [p] "v"(p), [z] "v"(zero), [mask] "i"(MASK) : "memory", "v160", "v161");
}
template <int MASK, int DEPTH> __device__ __forceinline__ void poll(GAS u64* p, u64 want, u32 thr, u32& cnt) {
  u64 sv; u64 zero = 0;
  asm volatile("s_mov_b64 %[sv], exec\n\ts_mov_b64 exec, %[mask]\n"
               "1:\n\t" PCHECK PCHECK PCHECK
               "v_cmp_gt_u32 vcc, %[thr], v160\n\ts_cbranch_vccz 3f\n\ts_sleep 6\n"
               "3:\n\ts_waitcnt vmcnt(%[depth])\n\tv_cmp_ne_u64 vcc, v[160:161], %[want]\n\ts_cbranch_vccz 2f\n\t" PISSUE "s_sub_u32 %[cnt], %[cnt], 1\n\ts_cbranch_scc0 1b\n"
               "2:\n\ts_mov_b64 exec, %[sv]"
               : [sv] "=&s"(sv), [cnt] "+s"(cnt) : [p] "v"(p), [z] "v"(zero), [want] "v"(want), [thr] "s"(thr), [mask] "i"(MASK), [depth] "i"(DEPTH - 1)
               : "memory", "vcc", "scc", "v160", "v161");
}
template <int MASK> __device__ __forceinline__ void count_up(GAS u64* p, u64 inc) {
  u64 sv;
  asm volatile("s_mov_b64 %0, exec\n\ts_mov_b64 exec, %3\n\tglobal_atomic_add_x2 %1, %2, off sc1\n\ts_mov_b64 exec, %0" : "=&s"(sv) : "v"(p), "v"(inc), "i"(MASK) : "memory");
}
__device__ __forceinline__ f32x4 mfma(u32x4 a, u32x4 b, f32x4 c) {
  return __builtin_amdgcn_mfma_f32_16x16x32_bf16(__builtin_bit_cast(bf16x8, a), __builtin_bit_cast(bf16x8, b), c, 0, 0, 0);
}
__device__ __forceinline__ u32x4 trfrag(u32 a, int rowbytes4) {
  s16x4 x = __builtin_amdgcn_ds_read_tr16_b64_v4i16((LAS s16x4*)(uintptr_t)a);
  s16x4 y = __builtin_amdgcn_ds_read_tr16_b64_v4i16((LAS s16x4*)(uintptr_t)(a + rowbytes4));
  u32x2 u = __builtin_bit_cast(u32x2, x), v = __builtin_bit_cast(u32x2, y);
  return u32x4{u[0], u[1], v[0], v[1]};
}
template <typename T> __device__ __forceinline__ T lds(u32 a) { return *(LAS T*)(uintptr_t)a; }
template <typename T> __device__ __forceinline__ void sts(u32 a, T v) { *(LAS T*)(uintptr_t)a = v; }
__device__ __forceinline__ float U2F(u32 x) { return __uint_as_float(x); }
__device__ __forceinline__ u32 F2U(float x) { return __float_as_uint(x); }
__device__ __forceinline__ float rbf(float f) { return (float)(__bf16)f; }
__device__ __forceinline__ u32 pk(float lo, float hi) { bf16x2 v = {(__bf16)lo, (__bf16)hi}; return __builtin_bit_cast(u32, v); }
__device__ __forceinline__ float blo(u32 w) { return U2F(w << 16); }
__device__ __forceinline__ float bhi(u32 w) { return U2F(w & 0xffff0000u); }
__device__ __forceinline__ float rcp(float x) { return __builtin_amdgcn_rcpf(x); }
__device__ __forceinline__ float ex2(float x) { return __builtin_amdgcn_exp2f(x); }
__device__ __forceinline__ float sigm(float z) { return rcp(1.f + ex2(-1.4426950408889634f * z)); }
template <int CTRL> __device__ __forceinline__ float dpp(float x) {
  return __builtin_bit_cast(float, __builtin_amdgcn_mov_dpp(__builtin_bit_cast(int, x), CTRL, 0xf, 0xf, true));
}
constexpr int XOR1 = 0xB1, XOR2 = 0x4E, ROR4 = 0x124, ROR8 = 0x128;
__device__ __forceinline__ float x16_sum(float x) { auto s = __builtin_amdgcn_permlane16_swap(F2U(x), F2U(x), false, false); return U2F(s[0]) + U2F(s[1]); }
__device__ __forceinline__ float x32_sum(float x) { auto s = __builtin_amdgcn_permlane32_swap(F2U(x), F2U(x), false, false); return U2F(s[0]) + U2F(s[1]); }
__device__ __forceinline__ float x16_max(float x) { auto s = __builtin_amdgcn_permlane16_swap(F2U(x), F2U(x), false, false); return fmaxf(U2F(s[0]), U2F(s[1])); }
__device__ __forceinline__ float x32_max(float x) { auto s = __builtin_amdgcn_permlane32_swap(F2U(x), F2U(x), false, false); return fmaxf(U2F(s[0]), U2F(s[1])); }
__device__ __forceinline__ u32x4 tag_bytes(u32 tag) { return u32x4{tag & 0xff, (tag >> 8) & 0xff, (tag >> 16) & 0xff, tag >> 24}; }
__device__ __forceinline__ u32x4 seal(f32x4 v, u32x4 tb) {
  return u32x4{(F2U(v[0]) & 0xffffff00u) | tb[0], (F2U(v[1]) & 0xffffff00u) | tb[1], (F2U(v[2]) & 0xffffff00u) | tb[2], (F2U(v[3]) & 0xffffff00u) | tb[3]};
}
__device__ __forceinline__ u32 unseal(u32x4& v, u32x4 tb) { v ^= tb; return v[0] | v[1] | v[2] | v[3]; }
#define BAD(x) __any(((x) & 0xff) != 0)
__device__ __forceinline__ f32x4 asf(u32x4 v) { return __builtin_bit_cast(f32x4, v); }
__device__ __forceinline__ u32 get_tag(const unsigned char* ws) {
  u32 ep;
  asm volatile("s_load_dword %0, %1, 0x0\n\ts_waitcnt lgkmcnt(0)" : "=s"(ep) : "s"(ws) : "memory");
  return ep + 0x3b5d2f47u;
}
#define BAR() asm volatile("s_waitcnt lgkmcnt(0)\n\ts_barrier" ::: "memory")
#define BAR0() do { BAR(); t0 = __builtin_amdgcn_readfirstlane(lds<u32>(L0 + C_T0)); } while (0)
template <int OFF> __device__ __forceinline__ void arrive(u32 l0, u32 one) {
  u64 sv;
  asm volatile("s_mov_b64 %0, exec\n\ts_mov_b64 exec, 1\n\tds_add_u32 %1, %2 offset:%3\n\ts_mov_b64 exec, %0" : "=&s"(sv) : "v"(l0), "v"(one), "i"(OFF) : "memory");
}
template <int OFF, int N, int SLEEP> __device__ __forceinline__ void waitn(u32 l0) {
  u32 t;
  asm volatile("s_branch 1f\n0:\n\ts_sleep %4\n1:\n\tds_read_b32 %0, %1 offset:%2\n\ts_waitcnt lgkmcnt(0)\n\tv_cmp_ne_u32 vcc, %3, %0\n\ts_cbranch_vccnz 0b"
               : "=&v"(t) : "v"(l0), "i"(OFF), "i"(N), "i"(SLEEP) : "memory", "vcc");
}
#define RETRY(cond, reload) \
  if (__builtin_expect(cond, 0)) { _Pragma("nounroll") for (int sp = 0; sp < SPIN_MAX; ++sp) { reload; asm volatile("" ::: "memory"); if (!(cond)) break; } }

__global__ void __launch_bounds__(768, 3) fused_decode(const u16*, const u16*, const u16*, const u16*, const u16*, const u16*, unsigned char*,
                                                       const u16*, const u16*, const u16*, u16*) {
  asm volatile("s_load_dwordx16 s[64:79], %0, 0x0\n\ts_load_dwordx4 s[80:83], %0, 0x40\n\ts_load_dwordx2 s[84:85], %0, 0x50"
               :: "s"(__builtin_amdgcn_kernarg_segment_ptr())
               : "memory", "s64", "s65", "s66", "s67", "s68", "s69", "s70", "s71", "s72", "s73", "s74", "s75", "s76", "s77", "s78", "s79", "s80", "s81",
                 "s82", "s83", "s84", "s85");
  __shared__ __attribute__((aligned(256))) unsigned char smem[L_END];
  const u32 L0 = (u32)(uintptr_t)(LAS unsigned char*)smem;
  const int w = blockIdx.x, tid = threadIdx.x, lane = tid & 63;
  int wv = __builtin_amdgcn_readfirstlane(tid >> 6);
  const int kvh = w & 7, r = w >> 3, b = r >> 3, s = r & 7;
  const int n = lane & 15, g = lane >> 4, q4 = n >> 2, p = n & 3;
  const int kq = r & 3, tau = kvh * 28 + r, cq = ((r >> 2) + 4) & 7;
  const bool has_f = r < 28;
  const u32 kvoff = (u32)(((b * 8 + kvh) * SK + (SK - WIN) + s * 256) * 256);
  u32 t0 = 0;
#define DMA4(base) "s_nop 0\n\tglobal_load_lds_dwordx4 %0, " base " nt\n\ts_add_u32 m0, m0, 0x400\n\ts_nop 0\n\tglobal_load_lds_dwordx4 %1, " base " nt\n\t" \
                   "s_add_u32 m0, m0, 0x400\n\ts_nop 0\n\tglobal_load_lds_dwordx4 %2, " base " nt\n\ts_add_u32 m0, m0, 0x400\n\ts_nop 0\n\tglobal_load_lds_dwordx4 %3, " base " nt"
  if (wv >= 8) {
  } else if (wv >= 4) {
    __builtin_amdgcn_s_setprio(2);
    const u32 k0 = kvoff + g * 256 + ((n ^ (4 * g)) << 4);
    BAR0();
#pragma unroll
    for (int t = 0; t < 4; ++t) {
      const u32 o = k0 + ((4 * t + wv + r) & 15) * 4096;
      gate(t0 + P0 + t * PACE + (wv - 4) * (PACE / 4));
      asm volatile("s_mov_b32 m0, %4\n\ts_nop 0\n\tglobal_load_lds_dwordx4 %0, s[70:71] nt\n\tglobal_load_lds_dwordx4 %1, s[70:71] offset:1024 nt\n\t"
                   "global_load_lds_dwordx4 %2, s[70:71] offset:2048 nt\n\tglobal_load_lds_dwordx4 %3, s[70:71] offset:3072 nt"
                   :: "v"(o), "v"(o ^ 16), "v"(o ^ 32), "v"(o ^ 48), "s"(L0 + L_K + (wv - 4) * 16384 + t * 4096) : "memory");
    }
  } else {
    const u32 row = 128 * kq + 32 * wv + (lane >> 3), c0 = ((lane & 7) ^ (((lane >> 4) & 1) << 1)) << 4;
    const u32 oq0 = row * 8192 + kvh * 1024 + cq * 128 + c0, oq1 = (oq0 ^ 64) + 8 * 8192;
    u32 of0 = row * 3584 + (tau >> 3) * 128 + c0, of1 = (of0 ^ 64) + 8 * 3584, of2 = of0 + 16 * 3584, of3 = of1 + 16 * 3584;
    u32 mf = L0 + L_G1 + 16384 + 4096 * wv;
    PIN("+v"(of0), "+v"(of1), "+v"(of2), "+v"(of3), "+s"(mf));
    asm volatile("s_mov_b32 m0, %4\n\ts_waitcnt lgkmcnt(0)\n\t" DMA4("s[64:65]") "\n\ts_mov_b32 m0, %6\n\ts_nop 0\n\tglobal_load_lds_dwordx4 %5, s[74:75]"
                 :: "v"(oq0), "v"(oq1), "v"(oq0 + 16 * 8192), "v"(oq1 + 16 * 8192), "s"(L0 + L_G1 + 4096 * wv), "v"(tid * 16), "s"(L0 + L_X + wv * XS) : "memory", "scc");
    if (has_f)
      asm volatile("s_bitcmp1_b32 %5, 2\n\ts_cselect_b64 s[66:67], s[68:69], s[66:67]\n\ts_mov_b32 m0, %4\n\t" DMA4("s[66:67]")
                   :: "v"(of0), "v"(of1), "v"(of2), "v"(of3), "s"(mf), "s"(tau) : "memory", "scc");
    __builtin_amdgcn_s_setprio(3);
    if (tid < 16) sts<u32>(L0 + L_CNT + tid * 4, tid == 7 ? (u32)__builtin_amdgcn_s_memrealtime() : 0u);
    BAR0();
  }
  const u16 *vc, *gw; unsigned char* ws; u16* out;
  asm volatile("s_waitcnt lgkmcnt(0)\n\ts_mov_b64 %0, s[72:73]\n\ts_mov_b64 %1, s[76:77]\n\ts_mov_b64 %2, s[78:79]\n\ts_mov_b64 %3, s[84:85]"
               : "=s"(vc), "=s"(ws), "=s"(gw), "=s"(out) :: "memory");
  asm volatile("" : "+s"(wv));
  u32 tag;
  const rsrc_t rw = mk(ws, WS_END);
  const int nb = r & 3, kb = 8 * kvh + (r >> 2), beta = 7 * kvh + (r >> 2);
  const bool has_w2 = (r >> 2) < 7;
  u32 vl0 = L0, one = 1; PIN("+v"(vl0), "+v"(one));

  if (wv >= 4 && wv < 8) {
    const int kw = wv - 4;
    tag = get_tag(ws);
    u32x4 tb = tag_bytes(tag);
    asm volatile("s_waitcnt vmcnt(0)" ::: "memory");
    const int bt = has_w2 ? beta : 0;
    const int fo = ((bt >> 1) & 7) * RG + WS_FB + ((((bt >> 4) * 8 + (lane >> 3)) * 4 + kw) * 16 + (bt & 1) * 8 + (lane & 7)) * 16;
    u32x4 f0 = ld<AUX_SC1>(rw, fo);
    u32x4 kf[4][4];
    {
      const u32 ka = L0 + L_K + kw * 16384 + (4 * p + q4) * 256 + ((g ^ n) << 4);
#pragma unroll
      for (int i = 0; i < 4; ++i)
#pragma unroll
        for (int t = 0; t < 4; ++t) kf[t][i] = lds<u32x4>((ka ^ (i << 6)) + t * 4096);
    }
    u32 qa = L0 + L_Q + p * QS + g * 16, pwa = L0 + L_P + kw * 512 + p * 128 + g * 32 + q4 * 8, vlp = L0 + kw * 4;
#pragma unroll
    for (int t = 0; t < 4; ++t) PIN("+v"(kf[t][0]), "+v"(kf[t][1]), "+v"(kf[t][2]), "+v"(kf[t][3]));
    PIN("+v"(qa), "+v"(pwa), "+v"(vlp));
    waitn<C_Q, 4, 1>(vl0);
    u32x4 qf[4];
#pragma unroll
    for (int i = 0; i < 4; ++i) qf[i] = lds<u32x4>(qa + i * 64);
    f32x4 sc[4];
#pragma unroll
    for (int t = 0; t < 4; ++t) sc[t] = mfma(kf[t][0], qf[0], f32x4{0.f, 0.f, 0.f, 0.f});
#pragma unroll
    for (int i = 1; i < 4; ++i)
#pragma unroll
      for (int t = 0; t < 4; ++t) sc[t] = mfma(kf[t][i], qf[i], sc[t]);
    float v[4];
#pragma unroll
    for (int j = 0; j < 4; ++j) { const float lo = (n & 4) ? sc[1][j] : sc[0][j], hi = (n & 4) ? sc[3][j] : sc[2][j]; v[j] = (n & 8) ? hi : lo; }
#pragma unroll
    for (int j = 0; j < 4; j += 2) {
      u32 t = pk(v[j], v[j + 1]);
      t = pk(blo(t) * 0.08838834764831845f, bhi(t) * 0.08838834764831845f);
      v[j] = blo(t) * 1.4426950408889634f; v[j + 1] = bhi(t) * 1.4426950408889634f;
    }
    float m = fmaxf(fmaxf(v[0], v[1]), fmaxf(v[2], v[3]));
    m = fmaxf(m, dpp<ROR4>(m)); m = fmaxf(m, dpp<ROR8>(m)); m = x16_max(m); m = x32_max(m);
#pragma unroll
    for (int j = 0; j < 4; ++j) v[j] = ex2(v[j] - m);
    sts<u32x2>(pwa, u32x2{pk(v[0], v[1]), pk(v[2], v[3])});
    arrive<C_P>(vlp, one);
    float l = (v[0] + v[1]) + (v[2] + v[3]);
    l += dpp<ROR4>(l); l += dpp<ROR8>(l); l = x16_sum(l); l = x32_sum(l);
    sts<float>(L0 + L_M + p * 16 + kw * 4, m);
    sts<float>(L0 + L_L + p * 16 + kw * 4, l);
    arrive<C_M>(vl0, one);
    asm volatile("" : "+v"(f0) :: "memory");
    RETRY(BAD(unseal(f0, tb)), f0 = ld<AUX_SC1>(rw, fo))
    {
      f32x4 a = asf(f0);
#pragma unroll
      for (int j = 0; j < 4; ++j) { a[j] += dpp<ROR8>(a[j]); a[j] = x16_sum(a[j]); }
      const u32 u0 = pk(a[0], a[1]), u1 = pk(a[2], a[3]);
      auto s0 = __builtin_amdgcn_permlane32_swap(u0, u0, false, false), s1 = __builtin_amdgcn_permlane32_swap(u1, u1, false, false);
      const u32 g0 = pk(blo(s0[0]) * sigm(blo(s0[0])), bhi(s0[0]) * sigm(bhi(s0[0]))), g1 = pk(blo(s1[0]) * sigm(blo(s1[0])), bhi(s1[0]) * sigm(bhi(s1[0])));
      sts<u32x2>(L0 + L_H + kw * HS + (lane & 7) * 8, u32x2{pk(blo(g0) * blo(s0[1]), bhi(g0) * bhi(s0[1])), pk(blo(g1) * blo(s1[1]), bhi(g1) * bhi(s1[1]))});
    }
    waitn<C_M, 4, 1>(vl0);
    f32x4 al; u32x4 mu;
    {
      const int hs = lane < 32 ? kw : p;
      const f32x4 mm = lds<f32x4>(L0 + L_M + hs * 16), ll = lds<f32x4>(L0 + L_L + hs * 16);
      const float M = fmaxf(fmaxf(mm[0], mm[1]), fmaxf(mm[2], mm[3]));
#pragma unroll
      for (int j = 0; j < 4; ++j) al[j] = ex2(mm[j] - M);
      mu = u32x4{F2U(M), tag, F2U((al[0] * ll[0] + al[1] * ll[1]) + (al[2] * ll[2] + al[3] * ll[3])), ~tag};
    }
    u32 keep = lane < 32 ? 0xffffff00u : 0u;
    if (lane < 32) mu = tb;
    int pa = lane < 32 ? kvh * RG + WS_PB + ((((kw * 2 + (lane >> 4)) * 4 + b) * 8 + s) * 16 + n) * 16 : (kw == 1 && lane < 36) ? kvh * RG + WS_MB + (r * 4 + p) * 16 : WS_END;
    u32 pr = L0 + L_PO + kw * 512 + (lane & 31) * 16;
    int po = kvh * RG + WS_PB + (((r >> 2) * 4 + kw) * 128 + lane) * 16;
    u32 ow = L0 + L_O + (g * 4 + kw) * OS + n * 8;
    u64 inc = 1;
    GAS u64* a3 = (GAS u64*)(ws + c3_at(kw == 0 ? (((w >> 3) & 3) * 4 + (lane & 3)) * 4 + (w & 3) : (nb * 4 + (kvh >> 1)) * 4 + (lane & 3)));
    const int cg = 4 * w + kw;
    int oo = (cg & 7) * RG + WS_OB + ((cg >> 3) * 256 + lane) * 16;
    int oe = n < 4 ? (p * 512 + 4 * cg + g) * 2 : 4096;
    u64 want = ((u64)tag << 32) | 16u;
    PIN("+v"(pa), "+v"(pr), "+v"(po), "+v"(mu), "+v"(keep), "+v"(ow), "+v"(inc), "+v"(al), "+v"(a3), "+v"(oo), "+v"(oe), "+v"(want), "+v"(tb));
    __builtin_amdgcn_s_setprio(3);
    arrive<C_V>(vl0, one);
    BAR();
    {
      const f32x4 o = (al[0] * lds<f32x4>(pr) + al[1] * lds<f32x4>(pr + 2048)) + (al[2] * lds<f32x4>(pr + 4096) + al[3] * lds<f32x4>(pr + 6144));
      st16(rw, pa, u32x4{(F2U(o[0]) & keep) | mu[0], (F2U(o[1]) & keep) | mu[1], (F2U(o[2]) & keep) | mu[2], (F2U(o[3]) & keep) | mu[3]});
    }
    waitn<C_GO, 1, 0>(vl0);
    u32x4 p0 = ld<AUX_SC1>(rw, po), p1 = ld<AUX_SC1>(rw, po + 1024);
    RETRY(BAD(unseal(p0, tb) | unseal(p1, tb)), (p0 = ld<AUX_SC1>(rw, po), p1 = ld<AUX_SC1>(rw, po + 1024)))
    sts<u32x2>(ow, u32x2{pk(U2F(p0[0]), U2F(p0[1])), pk(U2F(p0[2]), U2F(p0[3]))});
    sts<u32x2>(ow + 16 * OS, u32x2{pk(U2F(p1[0]), U2F(p1[1])), pk(U2F(p1[2]), U2F(p1[3]))});
    BAR();
    if (kw == 1) count_up<15>(a3, inc);
    if (w < 32) {
      u32 cnt = HINT_MAX;
      if (kw == 0) { poll_first<15>(a3); poll<15, 6>(a3, want, 0, cnt); arrive<C_GO>(vl0, one); } else waitn<C_GO, 2, 0>(vl0);
      u32x4 e0 = ld<AUX_SC1>(rw, oo), e1 = ld<AUX_SC1>(rw, oo + 1024), e2 = ld<AUX_SC1>(rw, oo + 2048), e3 = ld<AUX_SC1>(rw, oo + 3072);
      RETRY(BAD(unseal(e0, tb) | unseal(e1, tb) | unseal(e2, tb) | unseal(e3, tb)),
            (e0 = ld<AUX_SC1>(rw, oo), e1 = ld<AUX_SC1>(rw, oo + 1024), e2 = ld<AUX_SC1>(rw, oo + 2048), e3 = ld<AUX_SC1>(rw, oo + 3072)))
      const f32x4 e = (asf(e0) + asf(e1)) + (asf(e2) + asf(e3));
      auto s0 = __builtin_amdgcn_permlane32_swap(F2U(e[0]), F2U(e[2]), false, false), s1 = __builtin_amdgcn_permlane32_swap(F2U(e[1]), F2U(e[3]), false, false);
      auto s2 = __builtin_amdgcn_permlane16_swap(F2U(U2F(s0[0]) + U2F(s0[1])), F2U(U2F(s1[0]) + U2F(s1[1])), false, false);
      float f = U2F(s2[0]) + U2F(s2[1]);
      f += dpp<ROR8>(f); f += dpp<ROR4>(f);
      __builtin_amdgcn_raw_buffer_store_b16((u16)pk(f, f), mk(out, 4096), oe, 0, AUX_SC1);
      if (cg == 0) __builtin_amdgcn_raw_buffer_store_b32(tag, rw, lane == 0 ? 0 : WS_END, 0, AUX_SC1);
    }
    return;
  }
  int mo = kvh * RG + WS_MB + ((p * 8 + q4) * 4 + (r >> 3)) * 16;
  if (wv < 4) {
    tag = get_tag(ws);
    const u32x4 tb = tag_bytes(tag);
    u32 wp = L0 + L_G1 + (8 * g + q4) * 128 + (((2 * wv + (p >> 1)) ^ ((((q4 >> 1) & 1) | ((g & 1) << 1)) << 1)) << 4) + 8 * (p & 1);
    u32 xp = L0 + L_X + p * XS + kq * 256 + g * 16;
    int dq = n >= 4 ? WS_END : kvh * RG + WS_QB + ((p * 4 + kq) * 128 + cq * 16 + 4 * wv + g) * 16;
    int df = n >= 4 ? WS_END : ((tau >> 3) & 7) * RG + WS_FB + ((((tau >> 6) * 8 + (tau & 7)) * 4 + p) * 16 + 4 * wv + g) * 16;
    PIN("+v"(wp), "+v"(xp), "+v"(dq), "+v"(df));
    if (has_f) asm volatile("s_waitcnt vmcnt(4)" ::: "memory"); else asm volatile("s_waitcnt vmcnt(0)" ::: "memory");
    arrive<C_G1>(vl0, one);
    waitn<C_G1, 4, 0>(vl0);
    u32x4 xf[4];
    {
      u32x4 wf[4];
#pragma unroll
      for (int ks = 0; ks < 4; ++ks) { wf[ks] = trfrag(wp + ks * 4096, 512); xf[ks] = lds<u32x4>(xp + ks * 64); }
      __builtin_amdgcn_sched_barrier(0);
      f32x4 acc = mfma(wf[0], xf[0], f32x4{0.f, 0.f, 0.f, 0.f}), acd = mfma(wf[1], xf[1], f32x4{0.f, 0.f, 0.f, 0.f});
      acc = mfma(wf[2], xf[2], acc); acd = mfma(wf[3], xf[3], acd);
      st16(rw, dq, seal(acc + acd, tb));
    }
    if (has_f) {
      asm volatile("s_waitcnt vmcnt(1)" ::: "memory");
      arrive<C_F1>(vl0, one);
      waitn<C_F1, 4, 0>(vl0);
      u32x4 wf[4];
#pragma unroll
      for (int ks = 0; ks < 4; ++ks) wf[ks] = trfrag(wp + 16384 + ks * 4096, 512);
      __builtin_amdgcn_sched_barrier(0);
      f32x4 acc = mfma(wf[0], xf[0], f32x4{0.f, 0.f, 0.f, 0.f}), acd = mfma(wf[1], xf[1], f32x4{0.f, 0.f, 0.f, 0.f});
      acc = mfma(wf[2], xf[2], acc); acd = mfma(wf[3], xf[3], acd);
      st16(rw, df, seal(acc + acd, tb));
    }
    if (wv == 0 && r < 8)
      __builtin_amdgcn_raw_buffer_store_b64(u32x2{0u, tag}, rw, lane == 0 ? r * RG + WS_C2 + kvh * 256 : lane == 1 ? c3_at(w) : WS_END, 0, AUX_SC1);
    {
      const int qo = kvh * RG + WS_QB + ((b * 4 + (lane >> 5)) * 128 + wv * 32 + (lane & 31)) * 16;
      gate((u32)__builtin_amdgcn_s_memrealtime() + DQ);
      gate(t0 + T1);
      u32x4 q0 = ld<AUX_SC1>(rw, qo), q1 = ld<AUX_SC1>(rw, qo + 4096);
      u32x4 gwv = ld<0>(mk(gw, 1024), lane * 16);
      RETRY(BAD(unseal(q0, tb) | unseal(q1, tb)), (q0 = ld<AUX_SC1>(rw, qo), q1 = ld<AUX_SC1>(rw, qo + 4096)))
      {
        const f32x4 qs = asf(q0) + asf(q1);
        auto s0 = __builtin_amdgcn_permlane32_swap(F2U(qs[0]), F2U(qs[2]), false, false), s1 = __builtin_amdgcn_permlane32_swap(F2U(qs[1]), F2U(qs[3]), false, false);
        sts<u32>(L0 + L_Q + wv * QS + (lane & 31) * 8 + (lane >> 5) * 4, pk(U2F(s0[0]) + U2F(s0[1]), U2F(s1[0]) + U2F(s1[1])));
      }
      arrive<C_Q>(vl0, one);
      PIN("+v"(gwv));
      const u32x4 xs = lds<u32x4>(L0 + L_X + wv * XS + lane * 16);
      float z = 0.f;
#pragma unroll
      for (int j = 0; j < 4; ++j) { const u32 t = pk(blo(xs[j]) * blo(gwv[j]), bhi(xs[j]) * bhi(gwv[j])); z += blo(t); z += bhi(t); }
      z += dpp<XOR1>(z); z += dpp<XOR2>(z); z += dpp<ROR4>(z); z += dpp<ROR8>(z); z = x16_sum(z); z = x32_sum(z);
      sts<float>(L0 + L_G + wv * 4, rbf(sigm(rbf(z))));
    }
    __builtin_amdgcn_s_setprio(0);
    {
      const u32 c1 = ((lane & 15) ^ (g << 1)) << 4;
      const u32 o0 = (kb * 64 + 16 * wv + g) * 1024 + nb * 256, o2 = (beta * 32 + 8 * wv + g) * 1024 + nb * 256 + (c1 ^ ((wv & 1) << 7));
      gate(t0 + G2T + wv * G2S);
      asm volatile("s_mov_b32 m0, %4\n\t" DMA4("s[80:81]")
                   :: "v"(o0 + c1), "v"(o0 + 4096 + c1), "v"(o0 + 8192 + (c1 ^ 128)), "v"(o0 + 12288 + (c1 ^ 128)), "s"(L0 + L_G2 + 4096 * wv) : "memory", "scc");
      if (has_w2)
        asm volatile("s_mov_b32 m0, %2\n\ts_nop 0\n\tglobal_load_lds_dwordx4 %0, s[82:83] nt\n\ts_add_u32 m0, m0, 0x400\n\ts_nop 0\n\tglobal_load_lds_dwordx4 %1, s[82:83] nt"
                     :: "v"(o2), "v"(o2 + 4096), "s"(L0 + L_G2 + 16384 + 2048 * wv) : "memory", "scc");
    }
    __builtin_amdgcn_s_setprio(2);
    arrive<C_V>(vl0, one);
    BAR();
    asm volatile("s_waitcnt vmcnt(0)" ::: "memory");
    arrive<C_G2>(vl0, one);
  } else {
    const int vw = wv - 8;
    __builtin_amdgcn_s_setprio(1);
    const rsrc_t rv = mk((const unsigned char*)vc + kvoff, 65536);
    BAR0();
    tag = get_tag(ws);
    u32x4 vf[4][4];
#pragma unroll
    for (int t = 0; t < 4; ++t) {
      const int tile = ((4 * t + wv - 4 + r) & 15) * 4096;
      gate(t0 + P0 + (4 + t) * PACE + vw * (PACE / 4));
#pragma unroll
      for (int i = 0; i < 4; ++i) vf[t][i] = ld<AUX_NT>(rv, tile + lane * 16 + i * 1024);
      __builtin_amdgcn_sched_barrier(0);
    }
    const int hn = (n + q4) & 3;
    u32 pra = L0 + L_P + vw * 512 + hn * 128 + g * 32, vlp = L0 + vw * 4;
    u32 pw = L0 + L_PO + vw * 2048 + g * 512 + n * 32;
    GAS u64* ap = (GAS u64*)(ws + (lane & 7) * RG + WS_C2 + kvh * 256);
    GAS u64* cp = (GAS u64*)(ws + (r & 7) * RG + WS_C2 + kvh * 256);
    u64 want = ((u64)tag << 32) | 32u, inc = 1;
    f32x4 acc[8];
    u32x4 va[8], vb[8];
#define PERM(t, i, c) __builtin_amdgcn_perm(vf[t][(i) + 1][(c) >> 1], vf[t][i][(c) >> 1], ((c) & 1) ? 0x07060302u : 0x05040100u)
#pragma unroll
    for (int c = 0; c < 8; ++c) va[c] = u32x4{PERM(0, 0, c), PERM(0, 2, c), PERM(1, 0, c), PERM(1, 2, c)};
    PIN("+v"(va[0]), "+v"(va[1]), "+v"(va[2]), "+v"(va[3]), "+v"(va[4]), "+v"(va[5]), "+v"(va[6]), "+v"(va[7]), "+v"(pra), "+v"(vlp), "+v"(pw));
    PIN("+v"(ap), "+v"(cp), "+v"(want), "+v"(inc), "+v"(mo));
    __builtin_amdgcn_sched_barrier(0);
#pragma unroll
    for (int c = 0; c < 8; ++c) { vb[c][0] = PERM(2, 0, c); vb[c][1] = PERM(2, 2, c); vb[c][2] = vb[c][0]; vb[c][3] = vb[c][0]; }
    PIN("+v"(vb[0]), "+v"(vb[1]), "+v"(vb[2]), "+v"(vb[3]), "+v"(vb[4]), "+v"(vb[5]), "+v"(vb[6]), "+v"(vb[7]));
    __builtin_amdgcn_s_setprio(3);
    waitn<C_P, 1, 1>(vlp);
    u32x4 pf0 = lds<u32x4>(pra), pf1 = lds<u32x4>(pra + 16);
#pragma unroll
    for (int c = 0; c < 8; ++c) acc[c] = mfma(pf0, va[c], f32x4{0.f, 0.f, 0.f, 0.f});
    __builtin_amdgcn_sched_barrier(0);
#pragma unroll
    for (int c = 0; c < 8; ++c) vb[c][2] = PERM(3, 0, c);
    PIN("+v"(vb[0]), "+v"(vb[1]), "+v"(vb[2]), "+v"(vb[3]), "+v"(vb[4]), "+v"(vb[5]), "+v"(vb[6]), "+v"(vb[7]), "+v"(pf1));
    __builtin_amdgcn_sched_barrier(0);
    PIN("+v"(vf[3][2]), "+v"(vf[3][3]));
    arrive<C_V>(vl0, one);
#pragma unroll
    for (int c = 0; c < 8; ++c) { vb[c][3] = PERM(3, 2, c); acc[c] = mfma(pf1, vb[c], acc[c]); }
    sts<f32x4>(pw, f32x4{acc[0][0], acc[1][0], acc[2][0], acc[3][0]});
    sts<f32x4>(pw + 16, f32x4{acc[4][0], acc[5][0], acc[6][0], acc[7][0]});
    if (vw == 0) {
      waitn<C_V, 12, 0>(vl0);
      count_up<255>(ap, inc);
      poll_first<1>(cp);
      BAR();
      u32 cnt = HINT_MAX;
      poll<1, 8>(cp, want, 20, cnt);
      arrive<C_GO>(vl0, one);
      asm volatile("global_load_dwordx4 v[152:155], %0, %1 sc1\n\tglobal_load_dwordx4 v[156:159], %0, %1 offset:256 sc1" :: "v"(mo), "s"(ws)
                   : "memory", "v152", "v153", "v154", "v155", "v156", "v157", "v158", "v159");
    } else {
      BAR();
    }
    __builtin_amdgcn_s_setprio(2);
  }
  const int ct = wv < 4 ? wv : wv - 4;
  u32 wa0 = L0 + L_G2 + (8 * g + q4) * 256 + (((2 * ct + (p >> 1)) ^ ((q4 | ((g & 1) << 2)) << 1)) << 4) + 8 * (p & 1);
  u32 orr = L0 + L_O + n * OS + g * 16, sa = L0 + L_S + n * 8;
  int oa = n >= 4 ? WS_END : ((4 * ct + g) & 7) * RG + WS_OB + (((4 * nb + (ct >> 1)) * 64 + kb) * 4 + p) * 16;
  float gt = lds<float>(L0 + L_G + p * 4);
  const u32x4 hf = lds<u32x4>(L0 + L_H + p * HS + g * 16);
  const float og = n < 4 ? rbf(1.f - gt) : 0.f;
  u32x4 tb = tag_bytes(tag);
  waitn<C_G2, 4, 0>(vl0);
  f32x4 ab = {0.f, 0.f, 0.f, 0.f};
  if (has_w2) ab = mfma(trfrag(wa0 + 16384, 1024), hf, ab) * og;
  u32x4 wf0 = trfrag(wa0, 1024), wf1 = trfrag(wa0 + 8192, 1024);
  PIN("+v"(wf0), "+v"(wf1), "+v"(ab), "+v"(orr), "+v"(oa), "+v"(sa), "+v"(gt), "+v"(tb));
  if (wv == 8) {
    float ma, mb, la, lb; u32 bad;
    asm volatile("s_waitcnt vmcnt(0)\n\tv_xor_b32 %4, %5, v153\n\tv_mov_b32 %0, v152\n\tv_bitop3_b32 %4, %5, v157, %4 bitop3:0xbe\n\tv_mov_b32 %1, v156\n\t"
                 "v_bitop3_b32 %4, %6, v155, %4 bitop3:0xbe\n\tv_mov_b32 %2, v154\n\tv_bitop3_b32 %4, %6, v159, %4 bitop3:0xbe\n\tv_mov_b32 %3, v158"
                 : "=&v"(ma), "=&v"(mb), "=&v"(la), "=&v"(lb), "=&v"(bad) : "s"(tag), "s"(~tag) : "memory");
    if (__builtin_expect(__any(bad != 0), 0)) {
      _Pragma("nounroll") for (int sp = 0; sp < SPIN_MAX; ++sp) {
        const u32x4 m0 = ld<AUX_SC1>(rw, mo), m1 = ld<AUX_SC1>(rw, mo + 256);
        asm volatile("" ::: "memory");
        ma = U2F(m0[0]); mb = U2F(m1[0]); la = U2F(m0[2]); lb = U2F(m1[2]);
        if (!__any(((m0[1] ^ tag) | (m0[3] ^ ~tag) | (m1[1] ^ tag) | (m1[3] ^ ~tag)) != 0)) break;
      }
    }
    float M;
    asm volatile("v_max_f32 %0, %1, %2\n\ts_nop 1\n\tv_max_f32_dpp %0, %0, %0 row_ror:4 row_mask:0xf bank_mask:0xf\n\ts_nop 1\n\tv_max_f32_dpp %0, %0, %0 row_ror:8 row_mask:0xf bank_mask:0xf"
                 : "=&v"(M) : "v"(ma), "v"(mb));
    const float e0 = ex2(ma - M), e1 = ex2(mb - M);
    float Ls = e0 * la + e1 * lb; Ls += dpp<ROR4>(Ls); Ls += dpp<ROR8>(Ls);
    const float sc = gt * rcp(Ls);
    sts<f32x2>(sa, f32x2{e0 * sc, e1 * sc});
  }
  BAR();
  {
    const u32x4 a0 = lds<u32x4>(orr), a1 = lds<u32x4>(orr + 16 * OS), a2 = lds<u32x4>(orr + 64), a3 = lds<u32x4>(orr + 16 * OS + 64);
    const f32x2 sw = lds<f32x2>(sa);
    __builtin_amdgcn_sched_barrier(0);
    f32x4 d0 = mfma(wf0, a0, f32x4{0.f, 0.f, 0.f, 0.f}), d1 = mfma(wf0, a1, f32x4{0.f, 0.f, 0.f, 0.f});
    __builtin_amdgcn_sched_barrier(0);
    d0 = mfma(wf1, a2, d0); d1 = mfma(wf1, a3, d1);
    ab += sw[0] * d0; ab += sw[1] * d1;
  }
#pragma unroll
  for (int j = 0; j < 4; ++j) ab[j] += dpp<ROR4>(ab[j]);
#pragma unroll
  for (int j = 0; j < 4; ++j) ab[j] += dpp<ROR8>(ab[j]);
  st16(rw, oa, seal(ab, tb));
}

extern "C" void kernel_launch(void* const* d_in, const int* in_sizes, int n_in, void* d_out, int out_size, void* d_ws, size_t ws_size, hipStream_t stream) {
  fused_decode<<<256, 768, 0, stream>>>((const u16*)d_in[3], (const u16*)d_in[6], (const u16*)d_in[7], (const u16*)d_in[1], (const u16*)d_in[2], (const u16*)d_in[0],
      (unsigned char*)d_ws, (const u16*)d_in[9], (const u16*)d_in[4], (const u16*)d_in[8], (u16*)d_out);
}
```

```cpp
#include <hip/hip_runtime.h>
#include <stdint.h>

typedef unsigned short u16;
typedef unsigned u32;
typedef unsigned long long u64;
typedef u32 u32x4 __attribute__((ext_vector_type(4)));
typedef u32 u32x2 __attribute__((ext_vector_type(2)));
typedef float f32x4 __attribute__((ext_vector_type(4)));
typedef float f32x2 __attribute__((ext_vector_type(2)));
typedef short s16x4 __attribute__((ext_vector_type(4)));
typedef __bf16 bf16x8 __attribute__((ext_vector_type(8)));
typedef __bf16 bf16x2 __attribute__((ext_vector_type(2)));
#define LAS __attribute__((address_space(3)))
#define GAS __attribute__((address_space(1)))
typedef __amdgpu_buffer_rsrc_t rsrc_t;

constexpr int SK = 8192, WIN = 2048;
constexpr int PACE = 58;
constexpr int P0 = 80;
constexpr int G2T = P0 + 8 * PACE + 10, G2S = 3 * PACE / 8;
constexpr int T1 = 300, DQ = 90;
constexpr int RG = 266496;
constexpr int WS_QB = 4096, WS_FB = WS_QB + 32768, WS_MB = WS_FB + 32768, WS_C2 = WS_MB + 2048, WS_C3 = WS_C2 + 2048, WS_PB = WS_C3 + 2048,
              WS_OB = WS_PB + 65536, WS_END = 8 * RG;
static_assert(WS_OB + 65536 <= RG, "region map");
constexpr int L_PO = 0, L_P = 8192, L_X = L_P + 2048, XS = 1040, L_Q = L_X + 4 * XS, QS = 272, L_M = L_Q + 4 * QS, L_L = L_M + 64,
              L_O = L_L + 64, OS = 144, L_S = L_O + 32 * OS, L_H = L_S + 128, HS = 80, L_G = L_H + 4 * HS, L_CNT = L_G + 16,
              L_G2 = (L_CNT + 64 + 255) & ~255, L_G1 = L_G2 + 24576, L_K = L_G1 + 32768, L_END = L_K + 65536;
constexpr int C_G1 = L_CNT, C_Q = L_CNT + 4, C_M = L_CNT + 8, C_V = L_CNT + 12, C_G2 = L_CNT + 16, C_GO = L_CNT + 20, C_F1 = L_CNT + 24,
              C_T0 = L_CNT + 28, C_P = L_CNT + 32, C_PO = L_CNT + 48;
constexpr int AUX_NT = 2, AUX_SC1 = 16;
constexpr int SPIN_MAX = 1 << 20, HINT_MAX = 512;

#define PIN(...) asm volatile("" : __VA_ARGS__)

__device__ __forceinline__ int c3_at(int j) { return (j & 7) * RG + WS_C3 + (j >> 3) * 256; }
__device__ __forceinline__ void gate(u32 until) {
  asm volatile("1:\n\ts_memrealtime vcc\n\ts_waitcnt lgkmcnt(0)\n\ts_sub_i32 vcc_lo, vcc_lo, %0\n\ts_cmp_ge_i32 vcc_lo, 0\n\ts_cbranch_scc1 2f\n\ts_sleep 1\n\ts_branch 1b\n2:"
               :: "s"(until) : "memory", "vcc", "scc");
}
__device__ __forceinline__ rsrc_t mk(const void* p, u32 n) { return __builtin_amdgcn_make_buffer_rsrc((void*)p, 0, n, 0x00020000); }
template <int AUX> __device__ __forceinline__ u32x4 ld(rsrc_t r, int off) { return __builtin_amdgcn_raw_buffer_load_b128(r, off, 0, AUX); }
__device__ __forceinline__ void st16(rsrc_t r, int off, u32x4 v) { __builtin_amdgcn_raw_buffer_store_b128(v, r, off, 0, AUX_SC1); }
#define PISSUE "global_atomic_add_x2 v[160:161], %[p], %[z], off sc0 sc1\n\t"
#define PCHECK "s_sleep 1\n\tv_cmp_ne_u64 vcc, v[160:161], %[want]\n\ts_cbranch_vccz 2f\n\t"
template <int MASK> __device__ __forceinline__ void poll_first(GAS u64* p) {
  u64 sv; u64 zero = 0;
  asm volatile("s_mov_b64 %[sv], exec\n\ts_mov_b64 exec, %[mask]\n\tv_mov_b64 v[160:161], -1\n\t" PISSUE "s_mov_b64 exec, %[sv]"
               : [sv] "=&s"(sv) : [p] "v"(p), [z] "v"(zero), [mask] "i"(MASK) : "memory", "v160", "v161");
}
template <int MASK, int DEPTH> __device__ __forceinline__ void poll(GAS u64* p, u64 want, u32 thr, u32& cnt) {
  u64 sv; u64 zero = 0;
  asm volatile("s_mov_b64 %[sv], exec\n\ts_mov_b64 exec, %[mask]\n"
               "1:\n\t" PCHECK PCHECK PCHECK
               "v_cmp_gt_u32 vcc, %[thr], v160\n\ts_cbranch_vccz 3f\n\ts_sleep 6\n"
               "3:\n\ts_waitcnt vmcnt(%[depth])\n\tv_cmp_ne_u64 vcc, v[160:161], %[want]\n\ts_cbranch_vccz 2f\n\t" PISSUE "s_sub_u32 %[cnt], %[cnt], 1\n\ts_cbranch_scc0 1b\n"
               "2:\n\ts_mov_b64 exec, %[sv]"
               : [sv] "=&s"(sv), [cnt] "+s"(cnt) : [p] "v"(p), [z] "v"(zero), [want] "v"(want), [thr] "s"(thr), [mask] "i"(MASK), [depth] "i"(DEPTH - 1)
               : "memory", "vcc", "scc", "v160", "v161");
}
template <int MASK> __device__ __forceinline__ void count_up(GAS u64* p, u64 inc) {
  u64 sv;
  asm volatile("s_mov_b64 %0, exec\n\ts_mov_b64 exec, %3\n\tglobal_atomic_add_x2 %1, %2, off sc1\n\ts_mov_b64 exec, %0" : "=&s"(sv) : "v"(p), "v"(inc), "i"(MASK) : "memory");
}
__device__ __forceinline__ f32x4 mfma(u32x4 a, u32x4 b, f32x4 c) {
  return __builtin_amdgcn_mfma_f32_16x16x32_bf16(__builtin_bit_cast(bf16x8, a), __builtin_bit_cast(bf16x8, b), c, 0, 0, 0);
}
__device__ __forceinline__ u32x4 trfrag(u32 a, int rowbytes4) {
  s16x4 x = __builtin_amdgcn_ds_read_tr16_b64_v4i16((LAS s16x4*)(uintptr_t)a);
  s16x4 y = __builtin_amdgcn_ds_read_tr16_b64_v4i16((LAS s16x4*)(uintptr_t)(a + rowbytes4));
  u32x2 u = __builtin_bit_cast(u32x2, x), v = __builtin_bit_cast(u32x2, y);
  return u32x4{u[0], u[1], v[0], v[1]};
}
template <typename T> __device__ __forceinline__ T lds(u32 a) { return *(LAS T*)(uintptr_t)a; }
template <typename T> __device__ __forceinline__ void sts(u32 a, T v) { *(LAS T*)(uintptr_t)a = v; }
__device__ __forceinline__ float U2F(u32 x) { return __uint_as_float(x); }
__device__ __forceinline__ u32 F2U(float x) { return __float_as_uint(x); }
__device__ __forceinline__ float rbf(float f) { return (float)(__bf16)f; }
__device__ __forceinline__ u32 pk(float lo, float hi) { bf16x2 v = {(__bf16)lo, (__bf16)hi}; return __builtin_bit_cast(u32, v); }
__device__ __forceinline__ float blo(u32 w) { return U2F(w << 16); }
__device__ __forceinline__ float bhi(u32 w) { return U2F(w & 0xffff0000u); }
__device__ __forceinline__ float rcp(float x) { return __builtin_amdgcn_rcpf(x); }
__device__ __forceinline__ float ex2(float x) { return __builtin_amdgcn_exp2f(x); }
__device__ __forceinline__ float sigm(float z) { return rcp(1.f + ex2(-1.4426950408889634f * z)); }
template <int CTRL> __device__ __forceinline__ float dpp(float x) {
  return __builtin_bit_cast(float, __builtin_amdgcn_mov_dpp(__builtin_bit_cast(int, x), CTRL, 0xf, 0xf, true));
}
constexpr int XOR1 = 0xB1, XOR2 = 0x4E, ROR4 = 0x124, ROR8 = 0x128;
__device__ __forceinline__ float x16_sum(float x) { auto s = __builtin_amdgcn_permlane16_swap(F2U(x), F2U(x), false, false); return U2F(s[0]) + U2F(s[1]); }
__device__ __forceinline__ float x32_sum(float x) { auto s = __builtin_amdgcn_permlane32_swap(F2U(x), F2U(x), false, false); return U2F(s[0]) + U2F(s[1]); }
__device__ __forceinline__ float x16_max(float x) { auto s = __builtin_amdgcn_permlane16_swap(F2U(x), F2U(x), false, false); return fmaxf(U2F(s[0]), U2F(s[1])); }
__device__ __forceinline__ float x32_max(float x) { auto s = __builtin_amdgcn_permlane32_swap(F2U(x), F2U(x), false, false); return fmaxf(U2F(s[0]), U2F(s[1])); }
__device__ __forceinline__ u32x4 tag_bytes(u32 tag) { return u32x4{tag & 0xff, (tag >> 8) & 0xff, (tag >> 16) & 0xff, tag >> 24}; }
__device__ __forceinline__ u32x4 seal(f32x4 v, u32x4 tb) {
  return u32x4{(F2U(v[0]) & 0xffffff00u) | tb[0], (F2U(v[1]) & 0xffffff00u) | tb[1], (F2U(v[2]) & 0xffffff00u) | tb[2], (F2U(v[3]) & 0xffffff00u) | tb[3]};
}
__device__ __forceinline__ u32 unseal(u32x4& v, u32x4 tb) { v ^= tb; return v[0] | v[1] | v[2] | v[3]; }
#define BAD(x) __any(((x) & 0xff) != 0)
__device__ __forceinline__ f32x4 asf(u32x4 v) { return __builtin_bit_cast(f32x4, v); }
__device__ __forceinline__ u32 get_tag(const unsigned char* ws) {
  u32 ep;
  asm volatile("s_load_dword %0, %1, 0x0\n\ts_waitcnt lgkmcnt(0)" : "=s"(ep) : "s"(ws) : "memory");
  return ep + 0x3b5d2f47u;
}
#define BAR() asm volatile("s_waitcnt lgkmcnt(0)\n\ts_barrier" ::: "memory")
#define BAR0() do { BAR(); t0 = __builtin_amdgcn_readfirstlane(lds<u32>(L0 + C_T0)); } while (0)
template <int OFF> __device__ __forceinline__ void arrive(u32 l0, u32 one) {
  u64 sv;
  asm volatile("s_mov_b64 %0, exec\n\ts_mov_b64 exec, 1\n\tds_add_u32 %1, %2 offset:%3\n\ts_mov_b64 exec, %0" : "=&s"(sv) : "v"(l0), "v"(one), "i"(OFF) : "memory");
}
template <int OFF, int N, int SLEEP> __device__ __forceinline__ void waitn(u32 l0) {
  u32 t;
  asm volatile("s_branch 1f\n0:\n\ts_sleep %4\n1:\n\tds_read_b32 %0, %1 offset:%2\n\ts_waitcnt lgkmcnt(0)\n\tv_cmp_ne_u32 vcc, %3, %0\n\ts_cbranch_vccnz 0b"
               : "=&v"(t) : "v"(l0), "i"(OFF), "i"(N), "i"(SLEEP) : "memory", "vcc");
}
#define RETRY(cond, reload) \
  if (__builtin_expect(cond, 0)) { _Pragma("nounroll") for (int sp = 0; sp < SPIN_MAX; ++sp) { reload; asm volatile("" ::: "memory"); if (!(cond)) break; } }

__global__ void __launch_bounds__(768, 3) fused_decode(const u16*, const u16*, const u16*, const u16*, const u16*, const u16*, unsigned char*,
                                                       const u16*, const u16*, const u16*, u16*) {
  asm volatile("s_load_dwordx16 s[64:79], %0, 0x0\n\ts_load_dwordx4 s[80:83], %0, 0x40\n\ts_load_dwordx2 s[84:85], %0, 0x50"
               :: "s"(__builtin_amdgcn_kernarg_segment_ptr())
               : "memory", "s64", "s65", "s66", "s67", "s68", "s69", "s70", "s71", "s72", "s73", "s74", "s75", "s76", "s77", "s78", "s79", "s80", "s81",
                 "s82", "s83", "s84", "s85");
  __shared__ __attribute__((aligned(256))) unsigned char smem[L_END];
  const u32 L0 = (u32)(uintptr_t)(LAS unsigned char*)smem;
  const int w = blockIdx.x, tid = threadIdx.x, lane = tid & 63;
  int wv = __builtin_amdgcn_readfirstlane(tid >> 6);
  const int kvh = w & 7, r = w >> 3, b = r >> 3, s = r & 7;
  const int n = lane & 15, g = lane >> 4, q4 = n >> 2, p = n & 3;
  const int kq = r & 3, tau = kvh * 28 + r, cq = ((r >> 2) + 4) & 7;
  const bool has_f = r < 28;
  const u32 kvoff = (u32)(((b * 8 + kvh) * SK + (SK - WIN) + s * 256) * 256);
  u32 t0 = 0;
#define DMA4(base) "s_nop 0\n\tglobal_load_lds_dwordx4 %0, " base " nt\n\ts_add_u32 m0, m0, 0x400\n\ts_nop 0\n\tglobal_load_lds_dwordx4 %1, " base " nt\n\t" \
                   "s_add_u32 m0, m0, 0x400\n\ts_nop 0\n\tglobal_load_lds_dwordx4 %2, " base " nt\n\ts_add_u32 m0, m0, 0x400\n\ts_nop 0\n\tglobal_load_lds_dwordx4 %3, " base " nt"
  if (wv >= 8) {
  } else if (wv >= 4) {
    __builtin_amdgcn_s_setprio(2);
    const u32 k0 = kvoff + g * 256 + ((n ^ (4 * g)) << 4);
    BAR0();
#pragma unroll
    for (int t = 0; t < 4; ++t) {
      const u32 o = k0 + ((4 * t + wv + r) & 15) * 4096;
      gate(t0 + P0 + t * PACE + (wv - 4) * (PACE / 4));
      asm volatile("s_mov_b32 m0, %4\n\ts_nop 0\n\tglobal_load_lds_dwordx4 %0, s[70:71] nt\n\tglobal_load_lds_dwordx4 %1, s[70:71] offset:1024 nt\n\t"
                   "global_load_lds_dwordx4 %2, s[70:71] offset:2048 nt\n\tglobal_load_lds_dwordx4 %3, s[70:71] offset:3072 nt"
                   :: "v"(o), "v"(o ^ 16), "v"(o ^ 32), "v"(o ^ 48), "s"(L0 + L_K + (wv - 4) * 16384 + t * 4096) : "memory");
    }
  } else {
    const u32 row = 128 * kq + 32 * wv + (lane >> 3), c0 = ((lane & 7) ^ (((lane >> 4) & 1) << 1)) << 4;
    const u32 oq0 = row * 8192 + kvh * 1024 + cq * 128 + c0, oq1 = (oq0 ^ 64) + 8 * 8192;
    u32 of0 = row * 3584 + (tau >> 3) * 128 + c0, of1 = (of0 ^ 64) + 8 * 3584, of2 = of0 + 16 * 3584, of3 = of1 + 16 * 3584;
    u32 mf = L0 + L_G1 + 16384 + 4096 * wv;
    PIN("+v"(of0), "+v"(of1), "+v"(of2), "+v"(of3), "+s"(mf));
    asm volatile("s_mov_b32 m0, %4\n\ts_waitcnt lgkmcnt(0)\n\t" DMA4("s[64:65]") "\n\ts_mov_b32 m0, %6\n\ts_nop 0\n\tglobal_load_lds_dwordx4 %5, s[74:75]"
                 :: "v"(oq0), "v"(oq1), "v"(oq0 + 16 * 8192), "v"(oq1 + 16 * 8192), "s"(L0 + L_G1 + 4096 * wv), "v"(tid * 16), "s"(L0 + L_X + wv * XS) : "memory", "scc");
    if (has_f)
      asm volatile("s_bitcmp1_b32 %5, 2\n\ts_cselect_b64 s[66:67], s[68:69], s[66:67]\n\ts_mov_b32 m0, %4\n\t" DMA4("s[66:67]")
                   :: "v"(of0), "v"(of1), "v"(of2), "v"(of3), "s"(mf), "s"(tau) : "memory", "scc");
    __builtin_amdgcn_s_setprio(3);
    if (tid < 16) sts<u32>(L0 + L_CNT + tid * 4, tid == 7 ? (u32)__builtin_amdgcn_s_memrealtime() : 0u);
    BAR0();
  }
  const u16 *vc, *gw; unsigned char* ws; u16* out;
  asm volatile("s_waitcnt lgkmcnt(0)\n\ts_mov_b64 %0, s[72:73]\n\ts_mov_b64 %1, s[76:77]\n\ts_mov_b64 %2, s[78:79]\n\ts_mov_b64 %3, s[84:85]"
               : "=s"(vc), "=s"(ws), "=s"(gw), "=s"(out) :: "memory");
  asm volatile("" : "+s"(wv));
  u32 tag;
  const rsrc_t rw = mk(ws, WS_END);
  const int nb = r & 3, kb = 8 * kvh + (r >> 2), beta = 7 * kvh + (r >> 2);
  const bool has_w2 = (r >> 2) < 7;
  u32 vl0 = L0, one = 1; PIN("+v"(vl0), "+v"(one));

  if (wv >= 4 && wv < 8) {
    const int kw = wv - 4;
    tag = get_tag(ws);
    u32x4 tb = tag_bytes(tag);
    asm volatile("s_waitcnt vmcnt(0)" ::: "memory");
    const int bt = has_w2 ? beta : 0;
    const int fo = ((bt >> 1) & 7) * RG + WS_FB + ((((bt >> 4) * 8 + (lane >> 3)) * 4 + kw) * 16 + (bt & 1) * 8 + (lane & 7)) * 16;
    u32x4 f0 = ld<AUX_SC1>(rw, fo);
    u32x4 kf[4][4];
    {
      const u32 ka = L0 + L_K + kw * 16384 + (4 * p + q4) * 256 + ((g ^ n) << 4);
#pragma unroll
      for (int i = 0; i < 4; ++i)
#pragma unroll
        for (int t = 0; t < 4; ++t) kf[t][i] = lds<u32x4>((ka ^ (i << 6)) + t * 4096);
    }
    u32 qa = L0 + L_Q + p * QS + g * 16, pwa = L0 + L_P + kw * 512 + p * 128 + g * 32 + q4 * 8, vlp = L0 + kw * 4;
#pragma unroll
    for (int t = 0; t < 4; ++t) PIN("+v"(kf[t][0]), "+v"(kf[t][1]), "+v"(kf[t][2]), "+v"(kf[t][3]));
    PIN("+v"(qa), "+v"(pwa), "+v"(vlp));
    waitn<C_Q, 4, 1>(vl0);
    u32x4 qf[4];
#pragma unroll
    for (int i = 0; i < 4; ++i) qf[i] = lds<u32x4>(qa + i * 64);
    f32x4 sc[4];
#pragma unroll
    for (int t = 0; t < 4; ++t) sc[t] = mfma(kf[t][0], qf[0], f32x4{0.f, 0.f, 0.f, 0.f});
#pragma unroll
    for (int i = 1; i < 4; ++i)
#pragma unroll
      for (int t = 0; t < 4; ++t) sc[t] = mfma(kf[t][i], qf[i], sc[t]);
    float v[4];
#pragma unroll
    for (int j = 0; j < 4; ++j) { const float lo = (n & 4) ? sc[1][j] : sc[0][j], hi = (n & 4) ? sc[3][j] : sc[2][j]; v[j] = (n & 8) ? hi : lo; }
#pragma unroll
    for (int j = 0; j < 4; j += 2) {
      u32 t = pk(v[j], v[j + 1]);
      t = pk(blo(t) * 0.08838834764831845f, bhi(t) * 0.08838834764831845f);
      v[j] = blo(t) * 1.4426950408889634f; v[j + 1] = bhi(t) * 1.4426950408889634f;
    }
    float m = fmaxf(fmaxf(v[0], v[1]), fmaxf(v[2], v[3]));
    m = fmaxf(m, dpp<ROR4>(m)); m = fmaxf(m, dpp<ROR8>(m)); m = x16_max(m); m = x32_max(m);
#pragma unroll
    for (int j = 0; j < 4; ++j) v[j] = ex2(v[j] - m);
    sts<u32x2>(pwa, u32x2{pk(v[0], v[1]), pk(v[2], v[3])});
    arrive<C_P>(vlp, one);
    float l = (v[0] + v[1]) + (v[2] + v[3]);
    l += dpp<ROR4>(l); l += dpp<ROR8>(l); l = x16_sum(l); l = x32_sum(l);
    sts<float>(L0 + L_M + p * 16 + kw * 4, m);
    sts<float>(L0 + L_L + p * 16 + kw * 4, l);
    arrive<C_M>(vl0, one);
    asm volatile("" : "+v"(f0) :: "memory");
    RETRY(BAD(unseal(f0, tb)), f0 = ld<AUX_SC1>(rw, fo))
    {
      f32x4 a = asf(f0);
#pragma unroll
      for (int j = 0; j < 4; ++j) { a[j] += dpp<ROR8>(a[j]); a[j] = x16_sum(a[j]); }
      const u32 u0 = pk(a[0], a[1]), u1 = pk(a[2], a[3]);
      auto s0 = __builtin_amdgcn_permlane32_swap(u0, u0, false, false), s1 = __builtin_amdgcn_permlane32_swap(u1, u1, false, false);
      const u32 g0 = pk(blo(s0[0]) * sigm(blo(s0[0])), bhi(s0[0]) * sigm(bhi(s0[0]))), g1 = pk(blo(s1[0]) * sigm(blo(s1[0])), bhi(s1[0]) * sigm(bhi(s1[0])));
      sts<u32x2>(L0 + L_H + kw * HS + (lane & 7) * 8, u32x2{pk(blo(g0) * blo(s0[1]), bhi(g0) * bhi(s0[1])), pk(blo(g1) * blo(s1[1]), bhi(g1) * bhi(s1[1]))});
    }
    waitn<C_M, 4, 1>(vl0);
    f32x4 al; u32x4 mu;
    {
      const int hs = lane < 32 ? kw : p;
      const f32x4 mm = lds<f32x4>(L0 + L_M + hs * 16), ll = lds<f32x4>(L0 + L_L + hs * 16);
      const float M = fmaxf(fmaxf(mm[0], mm[1]), fmaxf(mm[2], mm[3]));
#pragma unroll
      for (int j = 0; j < 4; ++j) al[j] = ex2(mm[j] - M);
      mu = u32x4{F2U(M), tag, F2U((al[0] * ll[0] + al[1] * ll[1]) + (al[2] * ll[2] + al[3] * ll[3])), ~tag};
    }
    u32 keep = lane < 32 ? 0xffffff00u : 0u;
    if (lane < 32) mu = tb;
    int pa = lane < 32 ? kvh * RG + WS_PB + ((((kw * 2 + (lane >> 4)) * 4 + b) * 8 + s) * 16 + n) * 16 : (kw == 1 && lane < 36) ? kvh * RG + WS_MB + (r * 4 + p) * 16 : WS_END;
    u32 pr = L0 + L_PO + kw * 512 + (lane & 31) * 16;
    int po = kvh * RG + WS_PB + (((r >> 2) * 4 + kw) * 128 + lane) * 16;
    u32 ow = L0 + L_O + (g * 4 + kw) * OS + n * 8;
    u64 inc = 1;
    GAS u64* a3 = (GAS u64*)(ws + c3_at(kw == 0 ? (((w >> 3) & 3) * 4 + (lane & 3)) * 4 + (w & 3) : (nb * 4 + (kvh >> 1)) * 4 + (lane & 3)));
    const int cg = 4 * w + kw;
    int oo = (cg & 7) * RG + WS_OB + ((cg >> 3) * 256 + lane) * 16;
    int oe = n < 4 ? (p * 512 + 4 * cg + g) * 2 : 4096;
    u64 want = ((u64)tag << 32) | 16u;
    PIN("+v"(pa), "+v"(pr), "+v"(po), "+v"(mu), "+v"(keep), "+v"(ow), "+v"(inc), "+v"(al), "+v"(a3), "+v"(oo), "+v"(oe), "+v"(want), "+v"(tb));
    __builtin_amdgcn_s_setprio(3);
    arrive<C_V>(vl0, one);
    arrive<C_G2>(vl0, one);
    waitn<C_PO, 4, 0>(vl0);
    {
      const f32x4 o = (al[0] * lds<f32x4>(pr) + al[1] * lds<f32x4>(pr + 2048)) + (al[2] * lds<f32x4>(pr + 4096) + al[3] * lds<f32x4>(pr + 6144));
      st16(rw, pa, u32x4{(F2U(o[0]) & keep) | mu[0], (F2U(o[1]) & keep) | mu[1], (F2U(o[2]) & keep) | mu[2], (F2U(o[3]) & keep) | mu[3]});
    }
    waitn<C_GO, 1, 0>(vl0);
    u32x4 p0 = ld<AUX_SC1>(rw, po), p1 = ld<AUX_SC1>(rw, po + 1024);
    RETRY(BAD(unseal(p0, tb) | unseal(p1, tb)), (p0 = ld<AUX_SC1>(rw, po), p1 = ld<AUX_SC1>(rw, po + 1024)))
    sts<u32x2>(ow, u32x2{pk(U2F(p0[0]), U2F(p0[1])), pk(U2F(p0[2]), U2F(p0[3]))});
    sts<u32x2>(ow + 16 * OS, u32x2{pk(U2F(p1[0]), U2F(p1[1])), pk(U2F(p1[2]), U2F(p1[3]))});
    BAR();
    if (kw == 1) count_up<15>(a3, inc);
    if (w < 32) {
      u32 cnt = HINT_MAX;
      if (kw == 0) { poll_first<15>(a3); poll<15, 6>(a3, want, 0, cnt); arrive<C_GO>(vl0, one); } else waitn<C_GO, 2, 0>(vl0);
      u32x4 e0 = ld<AUX_SC1>(rw, oo), e1 = ld<AUX_SC1>(rw, oo + 1024), e2 = ld<AUX_SC1>(rw, oo + 2048), e3 = ld<AUX_SC1>(rw, oo + 3072);
      RETRY(BAD(unseal(e0, tb) | unseal(e1, tb) | unseal(e2, tb) | unseal(e3, tb)),
            (e0 = ld<AUX_SC1>(rw, oo), e1 = ld<AUX_SC1>(rw, oo + 1024), e2 = ld<AUX_SC1>(rw, oo + 2048), e3 = ld<AUX_SC1>(rw, oo + 3072)))
      const f32x4 e = (asf(e0) + asf(e1)) + (asf(e2) + asf(e3));
      auto s0 = __builtin_amdgcn_permlane32_swap(F2U(e[0]), F2U(e[2]), false, false), s1 = __builtin_amdgcn_permlane32_swap(F2U(e[1]), F2U(e[3]), false, false);
      auto s2 = __builtin_amdgcn_permlane16_swap(F2U(U2F(s0[0]) + U2F(s0[1])), F2U(U2F(s1[0]) + U2F(s1[1])), false, false);
      float f = U2F(s2[0]) + U2F(s2[1]);
      f += dpp<ROR8>(f); f += dpp<ROR4>(f);
      __builtin_amdgcn_raw_buffer_store_b16((u16)pk(f, f), mk(out, 4096), oe, 0, AUX_SC1);
      if (cg == 0) __builtin_amdgcn_raw_buffer_store_b32(tag, rw, lane == 0 ? 0 : WS_END, 0, AUX_SC1);
    }
    return;
  }
  int mo = kvh * RG + WS_MB + ((p * 8 + q4) * 4 + (r >> 3)) * 16;
  if (wv < 4) {
    tag = get_tag(ws);
    const u32x4 tb = tag_bytes(tag);
    u32 wp = L0 + L_G1 + (8 * g + q4) * 128 + (((2 * wv + (p >> 1)) ^ ((((q4 >> 1) & 1) | ((g & 1) << 1)) << 1)) << 4) + 8 * (p & 1);
    u32 xp = L0 + L_X + p * XS + kq * 256 + g * 16;
    int dq = n >= 4 ? WS_END : kvh * RG + WS_QB + ((p * 4 + kq) * 128 + cq * 16 + 4 * wv + g) * 16;
    int df = n >= 4 ? WS_END : ((tau >> 3) & 7) * RG + WS_FB + ((((tau >> 6) * 8 + (tau & 7)) * 4 + p) * 16 + 4 * wv + g) * 16;
    PIN("+v"(wp), "+v"(xp), "+v"(dq), "+v"(df));
    if (has_f) asm volatile("s_waitcnt vmcnt(4)" ::: "memory"); else asm volatile("s_waitcnt vmcnt(0)" ::: "memory");
    arrive<C_G1>(vl0, one);
    waitn<C_G1, 4, 0>(vl0);
    u32x4 xf[4];
    {
      u32x4 wf[4];
#pragma unroll
      for (int ks = 0; ks < 4; ++ks) { wf[ks] = trfrag(wp + ks * 4096, 512); xf[ks] = lds<u32x4>(xp + ks * 64); }
      __builtin_amdgcn_sched_barrier(0);
      f32x4 acc = mfma(wf[0], xf[0], f32x4{0.f, 0.f, 0.f, 0.f}), acd = mfma(wf[1], xf[1], f32x4{0.f, 0.f, 0.f, 0.f});
      acc = mfma(wf[2], xf[2], acc); acd = mfma(wf[3], xf[3], acd);
      st16(rw, dq, seal(acc + acd, tb));
    }
    if (has_f) {
      asm volatile("s_waitcnt vmcnt(1)" ::: "memory");
      arrive<C_F1>(vl0, one);
      waitn<C_F1, 4, 0>(vl0);
      u32x4 wf[4];
#pragma unroll
      for (int ks = 0; ks < 4; ++ks) wf[ks] = trfrag(wp + 16384 + ks * 4096, 512);
      __builtin_amdgcn_sched_barrier(0);
      f32x4 acc = mfma(wf[0], xf[0], f32x4{0.f, 0.f, 0.f, 0.f}), acd = mfma(wf[1], xf[1], f32x4{0.f, 0.f, 0.f, 0.f});
      acc = mfma(wf[2], xf[2], acc); acd = mfma(wf[3], xf[3], acd);
      st16(rw, df, seal(acc + acd, tb));
    }
    if (wv == 0 && r < 8)
      __builtin_amdgcn_raw_buffer_store_b64(u32x2{0u, tag}, rw, lane == 0 ? r * RG + WS_C2 + kvh * 256 : lane == 1 ? c3_at(w) : WS_END, 0, AUX_SC1);
    {
      const int qo = kvh * RG + WS_QB + ((b * 4 + (lane >> 5)) * 128 + wv * 32 + (lane & 31)) * 16;
      gate((u32)__builtin_amdgcn_s_memrealtime() + DQ);
      gate(t0 + T1);
      u32x4 q0 = ld<AUX_SC1>(rw, qo), q1 = ld<AUX_SC1>(rw, qo + 4096);
      u32x4 gwv = ld<0>(mk(gw, 1024), lane * 16);
      RETRY(BAD(unseal(q0, tb) | unseal(q1, tb)), (q0 = ld<AUX_SC1>(rw, qo), q1 = ld<AUX_SC1>(rw, qo + 4096)))
      {
        const f32x4 qs = asf(q0) + asf(q1);
        auto s0 = __builtin_amdgcn_permlane32_swap(F2U(qs[0]), F2U(qs[2]), false, false), s1 = __builtin_amdgcn_permlane32_swap(F2U(qs[1]), F2U(qs[3]), false, false);
        sts<u32>(L0 + L_Q + wv * QS + (lane & 31) * 8 + (lane >> 5) * 4, pk(U2F(s0[0]) + U2F(s0[1]), U2F(s1[0]) + U2F(s1[1])));
      }
      arrive<C_Q>(vl0, one);
      PIN("+v"(gwv));
      const u32x4 xs = lds<u32x4>(L0 + L_X + wv * XS + lane * 16);
      float z = 0.f;
#pragma unroll
      for (int j = 0; j < 4; ++j) { const u32 t = pk(blo(xs[j]) * blo(gwv[j]), bhi(xs[j]) * bhi(gwv[j])); z += blo(t); z += bhi(t); }
      z += dpp<XOR1>(z); z += dpp<XOR2>(z); z += dpp<ROR4>(z); z += dpp<ROR8>(z); z = x16_sum(z); z = x32_sum(z);
      sts<float>(L0 + L_G + wv * 4, rbf(sigm(rbf(z))));
    }
    __builtin_amdgcn_s_setprio(0);
    {
      const u32 c1 = ((lane & 15) ^ (g << 1)) << 4;
      const u32 o0 = (kb * 64 + 16 * wv + g) * 1024 + nb * 256, o2 = (beta * 32 + 8 * wv + g) * 1024 + nb * 256 + (c1 ^ ((wv & 1) << 7));
      gate(t0 + G2T + wv * G2S);
      asm volatile("s_mov_b32 m0, %4\n\t" DMA4("s[80:81]")
                   :: "v"(o0 + c1), "v"(o0 + 4096 + c1), "v"(o0 + 8192 + (c1 ^ 128)), "v"(o0 + 12288 + (c1 ^ 128)), "s"(L0 + L_G2 + 4096 * wv) : "memory", "scc");
      if (has_w2)
        asm volatile("s_mov_b32 m0, %2\n\ts_nop 0\n\tglobal_load_lds_dwordx4 %0, s[82:83] nt\n\ts_add_u32 m0, m0, 0x400\n\ts_nop 0\n\tglobal_load_lds_dwordx4 %1, s[82:83] nt"
                     :: "v"(o2), "v"(o2 + 4096), "s"(L0 + L_G2 + 16384 + 2048 * wv) : "memory", "scc");
    }
    __builtin_amdgcn_s_setprio(2);
    asm volatile("s_waitcnt vmcnt(0)" ::: "memory");
    arrive<C_G2>(vl0, one);
  } else {
    const int vw = wv - 8;
    __builtin_amdgcn_s_setprio(1);
    const rsrc_t rv = mk((const unsigned char*)vc + kvoff, 65536);
    BAR0();
    tag = get_tag(ws);
    u32x4 vf[4][4];
#pragma unroll
    for (int t = 0; t < 4; ++t) {
      const int tile = ((4 * t + wv - 4 + r) & 15) * 4096;
      gate(t0 + P0 + (4 + t) * PACE + vw * (PACE / 4));
#pragma unroll
      for (int i = 0; i < 4; ++i) vf[t][i] = ld<AUX_NT>(rv, tile + lane * 16 + i * 1024);
      __builtin_amdgcn_sched_barrier(0);
    }
    const int hn = (n + q4) & 3;
    u32 pra = L0 + L_P + vw * 512 + hn * 128 + g * 32, vlp = L0 + vw * 4;
    u32 pw = L0 + L_PO + vw * 2048 + g * 512 + n * 32;
    GAS u64* ap = (GAS u64*)(ws + (lane & 7) * RG + WS_C2 + kvh * 256);
    GAS u64* cp = (GAS u64*)(ws + (r & 7) * RG + WS_C2 + kvh * 256);
    u64 want = ((u64)tag << 32) | 32u, inc = 1;
    f32x4 acc[8];
    u32x4 va[8], vb[8];
#define PERM(t, i, c) __builtin_amdgcn_perm(vf[t][(i) + 1][(c) >> 1], vf[t][i][(c) >> 1], ((c) & 1) ? 0x07060302u : 0x05040100u)
#pragma unroll
    for (int c = 0; c < 8; ++c) va[c] = u32x4{PERM(0, 0, c), PERM(0, 2, c), PERM(1, 0, c), PERM(1, 2, c)};
    PIN("+v"(va[0]), "+v"(va[1]), "+v"(va[2]), "+v"(va[3]), "+v"(va[4]), "+v"(va[5]), "+v"(va[6]), "+v"(va[7]), "+v"(pra), "+v"(vlp), "+v"(pw));
    PIN("+v"(ap), "+v"(cp), "+v"(want), "+v"(inc), "+v"(mo));
    __builtin_amdgcn_sched_barrier(0);
#pragma unroll
    for (int c = 0; c < 8; ++c) { vb[c][0] = PERM(2, 0, c); vb[c][1] = PERM(2, 2, c); vb[c][2] = vb[c][0]; vb[c][3] = vb[c][0]; }
    PIN("+v"(vb[0]), "+v"(vb[1]), "+v"(vb[2]), "+v"(vb[3]), "+v"(vb[4]), "+v"(vb[5]), "+v"(vb[6]), "+v"(vb[7]));
    __builtin_amdgcn_s_setprio(3);
    waitn<C_P, 1, 1>(vlp);
    u32x4 pf0 = lds<u32x4>(pra), pf1 = lds<u32x4>(pra + 16);
#pragma unroll
    for (int c = 0; c < 8; ++c) acc[c] = mfma(pf0, va[c], f32x4{0.f, 0.f, 0.f, 0.f});
    __builtin_amdgcn_sched_barrier(0);
#pragma unroll
    for (int c = 0; c < 8; ++c) vb[c][2] = PERM(3, 0, c);
    PIN("+v"(vb[0]), "+v"(vb[1]), "+v"(vb[2]), "+v"(vb[3]), "+v"(vb[4]), "+v"(vb[5]), "+v"(vb[6]), "+v"(vb[7]), "+v"(pf1));
    __builtin_amdgcn_sched_barrier(0);
    PIN("+v"(vf[3][2]), "+v"(vf[3][3]));
    arrive<C_V>(vl0, one);
#pragma unroll
    for (int c = 0; c < 8; ++c) { vb[c][3] = PERM(3, 2, c); acc[c] = mfma(pf1, vb[c], acc[c]); }
    sts<f32x4>(pw, f32x4{acc[0][0], acc[1][0], acc[2][0], acc[3][0]});
    sts<f32x4>(pw + 16, f32x4{acc[4][0], acc[5][0], acc[6][0], acc[7][0]});
    arrive<C_PO>(vl0, one);
    if (vw == 0) {
      waitn<C_V, 8, 0>(vl0);
      count_up<255>(ap, inc);
      poll_first<1>(cp);
      u32 cnt = HINT_MAX;
      poll<1, 8>(cp, want, 20, cnt);
      arrive<C_GO>(vl0, one);
      asm volatile("global_load_dwordx4 v[152:155], %0, %1 sc1\n\tglobal_load_dwordx4 v[156:159], %0, %1 offset:256 sc1" :: "v"(mo), "s"(ws)
                   : "memory", "v152", "v153", "v154", "v155", "v156", "v157", "v158", "v159");
    }
    __builtin_amdgcn_s_setprio(2);
  }
  const int ct = wv < 4 ? wv : wv - 4;
  u32 wa0 = L0 + L_G2 + (8 * g + q4) * 256 + (((2 * ct + (p >> 1)) ^ ((q4 | ((g & 1) << 2)) << 1)) << 4) + 8 * (p & 1);
  u32 orr = L0 + L_O + n * OS + g * 16, sa = L0 + L_S + n * 8;
  int oa = n >= 4 ? WS_END : ((4 * ct + g) & 7) * RG + WS_OB + (((4 * nb + (ct >> 1)) * 64 + kb) * 4 + p) * 16;
  u32x4 tb = tag_bytes(tag);
  PIN("+v"(wa0), "+v"(orr), "+v"(sa), "+v"(oa), "+v"(tb));
  waitn<C_G2, 8, 1>(vl0);
  float gt = lds<float>(L0 + L_G + p * 4);
  const u32x4 hf = lds<u32x4>(L0 + L_H + p * HS + g * 16);
  const float og = n < 4 ? rbf(1.f - gt) : 0.f;
  f32x4 ab = {0.f, 0.f, 0.f, 0.f};
  if (has_w2) ab = mfma(trfrag(wa0 + 16384, 1024), hf, ab) * og;
  u32x4 wf0 = trfrag(wa0, 1024), wf1 = trfrag(wa0 + 8192, 1024);
  PIN("+v"(wf0), "+v"(wf1), "+v"(ab), "+v"(orr), "+v"(oa), "+v"(sa), "+v"(gt), "+v"(tb));
  if (wv == 8) {
    float ma, mb, la, lb; u32 bad;
    asm volatile("s_waitcnt vmcnt(0)\n\tv_xor_b32 %4, %5, v153\n\tv_mov_b32 %0, v152\n\tv_bitop3_b32 %4, %5, v157, %4 bitop3:0xbe\n\tv_mov_b32 %1, v156\n\t"
                 "v_bitop3_b32 %4, %6, v155, %4 bitop3:0xbe\n\tv_mov_b32 %2, v154\n\tv_bitop3_b32 %4, %6, v159, %4 bitop3:0xbe\n\tv_mov_b32 %3, v158"
                 : "=&v"(ma), "=&v"(mb), "=&v"(la), "=&v"(lb), "=&v"(bad) : "s"(tag), "s"(~tag) : "memory");
    if (__builtin_expect(__any(bad != 0), 0)) {
      _Pragma("nounroll") for (int sp = 0; sp < SPIN_MAX; ++sp) {
        const u32x4 m0 = ld<AUX_SC1>(rw, mo), m1 = ld<AUX_SC1>(rw, mo + 256);
        asm volatile("" ::: "memory");
        ma = U2F(m0[0]); mb = U2F(m1[0]); la = U2F(m0[2]); lb = U2F(m1[2]);
        if (!__any(((m0[1] ^ tag) | (m0[3] ^ ~tag) | (m1[1] ^ tag) | (m1[3] ^ ~tag)) != 0)) break;
      }
    }
    float M;
    asm volatile("v_max_f32 %0, %1, %2\n\ts_nop 1\n\tv_max_f32_dpp %0, %0, %0 row_ror:4 row_mask:0xf bank_mask:0xf\n\ts_nop 1\n\tv_max_f32_dpp %0, %0, %0 row_ror:8 row_mask:0xf bank_mask:0xf"
                 : "=&v"(M) : "v"(ma), "v"(mb));
    const float e0 = ex2(ma - M), e1 = ex2(mb - M);
    float Ls = e0 * la + e1 * lb; Ls += dpp<ROR4>(Ls); Ls += dpp<ROR8>(Ls);
    const float sc = gt * rcp(Ls);
    sts<f32x2>(sa, f32x2{e0 * sc, e1 * sc});
  }
  BAR();
  {
    const u32x4 a0 = lds<u32x4>(orr), a1 = lds<u32x4>(orr + 16 * OS), a2 = lds<u32x4>(orr + 64), a3 = lds<u32x4>(orr + 16 * OS + 64);
    const f32x2 sw = lds<f32x2>(sa);
    __builtin_amdgcn_sched_barrier(0);
    f32x4 d0 = mfma(wf0, a0, f32x4{0.f, 0.f, 0.f, 0.f}), d1 = mfma(wf0, a1, f32x4{0.f, 0.f, 0.f, 0.f});
    __builtin_amdgcn_sched_barrier(0);
    d0 = mfma(wf1, a2, d0); d1 = mfma(wf1, a3, d1);
    ab += sw[0] * d0; ab += sw[1] * d1;
  }
#pragma unroll
  for (int j = 0; j < 4; ++j) ab[j] += dpp<ROR4>(ab[j]);
#pragma unroll
  for (int j = 0; j < 4; ++j) ab[j] += dpp<ROR8>(ab[j]);
  st16(rw, oa, seal(ab, tb));
}

extern "C" void kernel_launch(void* const* d_in, const int* in_sizes, int n_in, void* d_out, int out_size, void* d_ws, size_t ws_size, hipStream_t stream) {
  fused_decode<<<256, 768, 0, stream>>>((const u16*)d_in[3], (const u16*)d_in[6], (const u16*)d_in[7], (const u16*)d_in[1], (const u16*)d_in[2], (const u16*)d_in[0],
      (unsigned char*)d_ws, (const u16*)d_in[9], (const u16*)d_in[4], (const u16*)d_in[8], (u16*)d_out);
}
```

```cpp
#include <hip/hip_runtime.h>
#include <stdint.h>

typedef unsigned short u16;
typedef unsigned u32;
typedef unsigned long long u64;
typedef u32 u32x4 __attribute__((ext_vector_type(4)));
typedef u32 u32x2 __attribute__((ext_vector_type(2)));
typedef float f32x4 __attribute__((ext_vector_type(4)));
typedef float f32x2 __attribute__((ext_vector_type(2)));
typedef short s16x4 __attribute__((ext_vector_type(4)));
typedef __bf16 bf16x8 __attribute__((ext_vector_type(8)));
typedef __bf16 bf16x2 __attribute__((ext_vector_type(2)));
#define LAS __attribute__((address_space(3)))
#define GAS __attribute__((address_space(1)))
typedef __amdgpu_buffer_rsrc_t rsrc_t;

constexpr int SK = 8192, WIN = 2048;
constexpr int PACE = 58;
constexpr int P0 = 80;
constexpr int G2T = P0 + 8 * PACE + 10, G2S = 3 * PACE / 8;
constexpr int T1 = 300, DQ = 90;
constexpr int RG = 266496;
constexpr int WS_QB = 4096, WS_FB = WS_QB + 32768, WS_MB = WS_FB + 32768, WS_C2 = WS_MB + 2048, WS_C3 = WS_C2 + 2048, WS_PB = WS_C3 + 2048,
              WS_OB = WS_PB + 65536, WS_END = 8 * RG;
static_assert(WS_OB + 65536 <= RG, "region map");
constexpr int L_PO = 0, L_P = 8192, L_X = L_P + 2048, XS = 1040, L_Q = L_X + 4 * XS, QS = 272, L_M = L_Q + 4 * QS, L_L = L_M + 64,
              L_O = L_L + 64, OS = 144, L_S = L_O + 32 * OS, L_H = L_S + 128, HS = 80, L_G = L_H + 4 * HS, L_CNT = L_G + 16,
              L_G2 = (L_CNT + 64 + 255) & ~255, L_G1 = L_G2 + 24576, L_K = L_G1 + 32768, L_END = L_K + 65536;
constexpr int C_G1 = L_CNT, C_Q = L_CNT + 4, C_M = L_CNT + 8, C_V = L_CNT + 12, C_G2 = L_CNT + 16, C_GO = L_CNT + 20, C_F1 = L_CNT + 24,
              C_T0 = L_CNT + 28, C_P = L_CNT + 32;
constexpr int AUX_NT = 2, AUX_SC1 = 16;
constexpr int SPIN_MAX = 1 << 20, HINT_MAX = 512;

#define PIN(...) asm volatile("" : __VA_ARGS__)

__device__ __forceinline__ int c3_at(int j) { return (j & 7) * RG + WS_C3 + (j >> 3) * 256; }
__device__ __forceinline__ void gate(u32 until) {
  asm volatile("1:\n\ts_memrealtime vcc\n\ts_waitcnt lgkmcnt(0)\n\ts_sub_i32 vcc_lo, vcc_lo, %0\n\ts_cmp_ge_i32 vcc_lo, 0\n\ts_cbranch_scc1 2f\n\ts_sleep 1\n\ts_branch 1b\n2:"
               :: "s"(until) : "memory", "vcc", "scc");
}
__device__ __forceinline__ rsrc_t mk(const void* p, u32 n) { return __builtin_amdgcn_make_buffer_rsrc((void*)p, 0, n, 0x00020000); }
template <int AUX> __device__ __forceinline__ u32x4 ld(rsrc_t r, int off) { return __builtin_amdgcn_raw_buffer_load_b128(r, off, 0, AUX); }
__device__ __forceinline__ void st16(rsrc_t r, int off, u32x4 v) { __builtin_amdgcn_raw_buffer_store_b128(v, r, off, 0, AUX_SC1); }
#define PISSUE "global_atomic_add_x2 v[160:161], %[p], %[z], off sc0 sc1\n\t"
#define PCHECK "s_sleep 1\n\tv_cmp_ne_u64 vcc, v[160:161], %[want]\n\ts_cbranch_vccz 2f\n\t"
template <int MASK> __device__ __forceinline__ void poll_first(GAS u64* p) {
  u64 sv; u64 zero = 0;
  asm volatile("s_mov_b64 %[sv], exec\n\ts_mov_b64 exec, %[mask]\n\tv_mov_b64 v[160:161], -1\n\t" PISSUE "s_mov_b64 exec, %[sv]"
               : [sv] "=&s"(sv) : [p] "v"(p), [z] "v"(zero), [mask] "i"(MASK) : "memory", "v160", "v161");
}
template <int MASK, int DEPTH> __device__ __forceinline__ void poll(GAS u64* p, u64 want, u32 thr, u32& cnt) {
  u64 sv; u64 zero = 0;
  asm volatile("s_mov_b64 %[sv], exec\n\ts_mov_b64 exec, %[mask]\n"
               "1:\n\t" PCHECK PCHECK PCHECK
               "v_cmp_gt_u32 vcc, %[thr], v160\n\ts_cbranch_vccz 3f\n\ts_sleep 6\n"
               "3:\n\ts_waitcnt vmcnt(%[depth])\n\tv_cmp_ne_u64 vcc, v[160:161], %[want]\n\ts_cbranch_vccz 2f\n\t" PISSUE "s_sub_u32 %[cnt], %[cnt], 1\n\ts_cbranch_scc0 1b\n"
               "2:\n\ts_mov_b64 exec, %[sv]"
               : [sv] "=&s"(sv), [cnt] "+s"(cnt) : [p] "v"(p), [z] "v"(zero), [want] "v"(want), [thr] "s"(thr), [mask] "i"(MASK), [depth] "i"(DEPTH - 1)
               : "memory", "vcc", "scc", "v160", "v161");
}
template <int MASK> __device__ __forceinline__ void count_up(GAS u64* p, u64 inc) {
  u64 sv;
  asm volatile("s_mov_b64 %0, exec\n\ts_mov_b64 exec, %3\n\tglobal_atomic_add_x2 %1, %2, off sc1\n\ts_mov_b64 exec, %0" : "=&s"(sv) : "v"(p), "v"(inc), "i"(MASK) : "memory");
}
__device__ __forceinline__ f32x4 mfma(u32x4 a, u32x4 b, f32x4 c) {
  return __builtin_amdgcn_mfma_f32_16x16x32_bf16(__builtin_bit_cast(bf16x8, a), __builtin_bit_cast(bf16x8, b), c, 0, 0, 0);
}
__device__ __forceinline__ u32x4 trfrag(u32 a, int rowbytes4) {
  s16x4 x = __builtin_amdgcn_ds_read_tr16_b64_v4i16((LAS s16x4*)(uintptr_t)a);
  s16x4 y = __builtin_amdgcn_ds_read_tr16_b64_v4i16((LAS s16x4*)(uintptr_t)(a + rowbytes4));
  u32x2 u = __builtin_bit_cast(u32x2, x), v = __builtin_bit_cast(u32x2, y);
  return u32x4{u[0], u[1], v[0], v[1]};
}
template <typename T> __device__ __forceinline__ T lds(u32 a) { return *(LAS T*)(uintptr_t)a; }
template <typename T> __device__ __forceinline__ void sts(u32 a, T v) { *(LAS T*)(uintptr_t)a = v; }
__device__ __forceinline__ float U2F(u32 x) { return __uint_as_float(x); }
__device__ __forceinline__ u32 F2U(float x) { return __float_as_uint(x); }
__device__ __forceinline__ float rbf(float f) { return (float)(__bf16)f; }
__device__ __forceinline__ u32 pk(float lo, float hi) { bf16x2 v = {(__bf16)lo, (__bf16)hi}; return __builtin_bit_cast(u32, v); }
__device__ __forceinline__ float blo(u32 w) { return U2F(w << 16); }
__device__ __forceinline__ float bhi(u32 w) { return U2F(w & 0xffff0000u); }
__device__ __forceinline__ float rcp(float x) { return __builtin_amdgcn_rcpf(x); }
__device__ __forceinline__ float ex2(float x) { return __builtin_amdgcn_exp2f(x); }
__device__ __forceinline__ float sigm(float z) { return rcp(1.f + ex2(-1.4426950408889634f * z)); }
template <int CTRL> __device__ __forceinline__ float dpp(float x) {
  return __builtin_bit_cast(float, __builtin_amdgcn_mov_dpp(__builtin_bit_cast(int, x), CTRL, 0xf, 0xf, true));
}
constexpr int XOR1 = 0xB1, XOR2 = 0x4E, ROR4 = 0x124, ROR8 = 0x128;
__device__ __forceinline__ float x16_sum(float x) { auto s = __builtin_amdgcn_permlane16_swap(F2U(x), F2U(x), false, false); return U2F(s[0]) + U2F(s[1]); }
__device__ __forceinline__ float x32_sum(float x) { auto s = __builtin_amdgcn_permlane32_swap(F2U(x), F2U(x), false, false); return U2F(s[0]) + U2F(s[1]); }
__device__ __forceinline__ float x16_max(float x) { auto s = __builtin_amdgcn_permlane16_swap(F2U(x), F2U(x), false, false); return fmaxf(U2F(s[0]), U2F(s[1])); }
__device__ __forceinline__ float x32_max(float x) { auto s = __builtin_amdgcn_permlane32_swap(F2U(x), F2U(x), false, false); return fmaxf(U2F(s[0]), U2F(s[1])); }
__device__ __forceinline__ u32x4 tag_bytes(u32 tag) { return u32x4{tag & 0xff, (tag >> 8) & 0xff, (tag >> 16) & 0xff, tag >> 24}; }
__device__ __forceinline__ u32x4 seal(f32x4 v, u32x4 tb) {
  return u32x4{(F2U(v[0]) & 0xffffff00u) | tb[0], (F2U(v[1]) & 0xffffff00u) | tb[1], (F2U(v[2]) & 0xffffff00u) | tb[2], (F2U(v[3]) & 0xffffff00u) | tb[3]};
}
__device__ __forceinline__ u32x4 seal_rn(f32x4 v, u32x4 tb) {
  return seal(__builtin_bit_cast(f32x4, __builtin_bit_cast(u32x4, v) + 0x80u), tb);
}
__device__ __forceinline__ u32 unseal(u32x4& v, u32x4 tb) { v ^= tb; return v[0] | v[1] | v[2] | v[3]; }
#define BAD(x) __any(((x) & 0xff) != 0)
__device__ __forceinline__ f32x4 asf(u32x4 v) { return __builtin_bit_cast(f32x4, v); }
__device__ __forceinline__ u32 get_tag(const unsigned char* ws) {
  u32 ep;
  asm volatile("s_load_dword %0, %1, 0x0\n\ts_waitcnt lgkmcnt(0)" : "=s"(ep) : "s"(ws) : "memory");
  return ep + 0x3b5d2f47u;
}
#define BAR() asm volatile("s_waitcnt lgkmcnt(0)\n\ts_barrier" ::: "memory")
#define BAR0() do { BAR(); t0 = __builtin_amdgcn_readfirstlane(lds<u32>(L0 + C_T0)); } while (0)
template <int OFF> __device__ __forceinline__ void arrive(u32 l0, u32 one) {
  u64 sv;
  asm volatile("s_mov_b64 %0, exec\n\ts_mov_b64 exec, 1\n\tds_add_u32 %1, %2 offset:%3\n\ts_mov_b64 exec, %0" : "=&s"(sv) : "v"(l0), "v"(one), "i"(OFF) : "memory");
}
template <int OFF, int N, int SLEEP> __device__ __forceinline__ void waitn(u32 l0) {
  u32 t;
  asm volatile("s_branch 1f\n0:\n\ts_sleep %4\n1:\n\tds_read_b32 %0, %1 offset:%2\n\ts_waitcnt lgkmcnt(0)\n\tv_cmp_ne_u32 vcc, %3, %0\n\ts_cbranch_vccnz 0b"
               : "=&v"(t) : "v"(l0), "i"(OFF), "i"(N), "i"(SLEEP) : "memory", "vcc");
}
#define RETRY(cond, reload) \
  if (__builtin_expect(cond, 0)) { _Pragma("nounroll") for (int sp = 0; sp < SPIN_MAX; ++sp) { reload; asm volatile("" ::: "memory"); if (!(cond)) break; } }

__global__ void __launch_bounds__(768, 3) fused_decode(const u16*, const u16*, const u16*, const u16*, const u16*, const u16*, unsigned char*,
                                                       const u16*, const u16*, const u16*, u16*) {
  asm volatile("s_load_dwordx16 s[64:79], %0, 0x0\n\ts_load_dwordx4 s[80:83], %0, 0x40\n\ts_load_dwordx2 s[84:85], %0, 0x50"
               :: "s"(__builtin_amdgcn_kernarg_segment_ptr())
               : "memory", "s64", "s65", "s66", "s67", "s68", "s69", "s70", "s71", "s72", "s73", "s74", "s75", "s76", "s77", "s78", "s79", "s80", "s81",
                 "s82", "s83", "s84", "s85");
  __shared__ __attribute__((aligned(256))) unsigned char smem[L_END];
  const u32 L0 = (u32)(uintptr_t)(LAS unsigned char*)smem;
  const int w = blockIdx.x, tid = threadIdx.x, lane = tid & 63;
  int wv = __builtin_amdgcn_readfirstlane(tid >> 6);
  const int kvh = w & 7, r = w >> 3, b = r >> 3, s = r & 7;
  const int n = lane & 15, g = lane >> 4, q4 = n >> 2, p = n & 3;
  const int kq = r & 3, tau = kvh * 28 + r, cq = ((r >> 2) + 4) & 7;
  const bool has_f = r < 28;
  const u32 kvoff = (u32)(((b * 8 + kvh) * SK + (SK - WIN) + s * 256) * 256);
  u32 t0 = 0;
#define DMA4(base) "s_nop 0\n\tglobal_load_lds_dwordx4 %0, " base " nt\n\ts_add_u32 m0, m0, 0x400\n\ts_nop 0\n\tglobal_load_lds_dwordx4 %1, " base " nt\n\t" \
                   "s_add_u32 m0, m0, 0x400\n\ts_nop 0\n\tglobal_load_lds_dwordx4 %2, " base " nt\n\ts_add_u32 m0, m0, 0x400\n\ts_nop 0\n\tglobal_load_lds_dwordx4 %3, " base " nt"
  if (wv >= 8) {
  } else if (wv >= 4) {
    __builtin_amdgcn_s_setprio(2);
    const u32 k0 = kvoff + g * 256 + ((n ^ (4 * g)) << 4);
    BAR0();
#pragma unroll
    for (int t = 0; t < 4; ++t) {
      const u32 o = k0 + ((4 * t + wv + r) & 15) * 4096;
      gate(t0 + P0 + t * PACE + (wv - 4) * (PACE / 4));
      asm volatile("s_mov_b32 m0, %4\n\ts_nop 0\n\tglobal_load_lds_dwordx4 %0, s[70:71] nt\n\tglobal_load_lds_dwordx4 %1, s[70:71] offset:1024 nt\n\t"
                   "global_load_lds_dwordx4 %2, s[70:71] offset:2048 nt\n\tglobal_load_lds_dwordx4 %3, s[70:71] offset:3072 nt"
                   :: "v"(o), "v"(o ^ 16), "v"(o ^ 32), "v"(o ^ 48), "s"(L0 + L_K + (wv - 4) * 16384 + t * 4096) : "memory");
    }
  } else {
    const u32 row = 128 * kq + 32 * wv + (lane >> 3), c0 = ((lane & 7) ^ (((lane >> 4) & 1) << 1)) << 4;
    const u32 oq0 = row * 8192 + kvh * 1024 + cq * 128 + c0, oq1 = (oq0 ^ 64) + 8 * 8192;
    u32 of0 = row * 3584 + (tau >> 3) * 128 + c0, of1 = (of0 ^ 64) + 8 * 3584, of2 = of0 + 16 * 3584, of3 = of1 + 16 * 3584;
    u32 mf = L0 + L_G1 + 16384 + 4096 * wv;
    PIN("+v"(of0), "+v"(of1), "+v"(of2), "+v"(of3), "+s"(mf));
    asm volatile("s_mov_b32 m0, %4\n\ts_waitcnt lgkmcnt(0)\n\t" DMA4("s[64:65]") "\n\ts_mov_b32 m0, %6\n\ts_nop 0\n\tglobal_load_lds_dwordx4 %5, s[74:75]"
                 :: "v"(oq0), "v"(oq1), "v"(oq0 + 16 * 8192), "v"(oq1 + 16 * 8192), "s"(L0 + L_G1 + 4096 * wv), "v"(tid * 16), "s"(L0 + L_X + wv * XS) : "memory", "scc");
    if (has_f)
      asm volatile("s_bitcmp1_b32 %5, 2\n\ts_cselect_b64 s[66:67], s[68:69], s[66:67]\n\ts_mov_b32 m0, %4\n\t" DMA4("s[66:67]")
                   :: "v"(of0), "v"(of1), "v"(of2), "v"(of3), "s"(mf), "s"(tau) : "memory", "scc");
    __builtin_amdgcn_s_setprio(3);
    if (tid < 16) sts<u32>(L0 + L_CNT + tid * 4, tid == 7 ? (u32)__builtin_amdgcn_s_memrealtime() : 0u);
    BAR0();
  }
  const u16 *vc, *gw; unsigned char* ws; u16* out;
  asm volatile("s_waitcnt lgkmcnt(0)\n\ts_mov_b64 %0, s[72:73]\n\ts_mov_b64 %1, s[76:77]\n\ts_mov_b64 %2, s[78:79]\n\ts_mov_b64 %3, s[84:85]"
               : "=s"(vc), "=s"(ws), "=s"(gw), "=s"(out) :: "memory");
  asm volatile("" : "+s"(wv));
  u32 tag;
  const rsrc_t rw = mk(ws, WS_END);
  const int nb = r & 3, kb = 8 * kvh + (r >> 2), beta = 7 * kvh + (r >> 2);
  const bool has_w2 = (r >> 2) < 7;
  u32 vl0 = L0, one = 1; PIN("+v"(vl0), "+v"(one));

  if (wv >= 4 && wv < 8) {
    const int kw = wv - 4;
    tag = get_tag(ws);
    u32x4 tb = tag_bytes(tag);
    asm volatile("s_waitcnt vmcnt(0)" ::: "memory");
    const int bt = has_w2 ? beta : 0;
    const int fo = ((bt >> 1) & 7) * RG + WS_FB + ((((bt >> 4) * 8 + (lane >> 3)) * 4 + kw) * 16 + (bt & 1) * 8 + (lane & 7)) * 16;
    u32x4 f0 = ld<AUX_SC1>(rw, fo);
    u32x4 kf[4][4];
    {
      const u32 ka = L0 + L_K + kw * 16384 + (4 * p + q4) * 256 + ((g ^ n) << 4);
#pragma unroll
      for (int i = 0; i < 4; ++i)
#pragma unroll
        for (int t = 0; t < 4; ++t) kf[t][i] = lds<u32x4>((ka ^ (i << 6)) + t * 4096);
    }
    u32 qa = L0 + L_Q + p * QS + g * 16, pwa = L0 + L_P + kw * 512 + p * 128 + g * 32 + q4 * 8, vlp = L0 + kw * 4;
#pragma unroll
    for (int t = 0; t < 4; ++t) PIN("+v"(kf[t][0]), "+v"(kf[t][1]), "+v"(kf[t][2]), "+v"(kf[t][3]));
    PIN("+v"(qa), "+v"(pwa), "+v"(vlp));
    waitn<C_Q, 4, 1>(vl0);
    u32x4 qf[4];
#pragma unroll
    for (int i = 0; i < 4; ++i) qf[i] = lds<u32x4>(qa + i * 64);
    f32x4 sc[4];
#pragma unroll
    for (int t = 0; t < 4; ++t) sc[t] = mfma(kf[t][0], qf[0], f32x4{0.f, 0.f, 0.f, 0.f});
#pragma unroll
    for (int i = 1; i < 4; ++i)
#pragma unroll
      for (int t = 0; t < 4; ++t) sc[t] = mfma(kf[t][i], qf[i], sc[t]);
    float v[4];
#pragma unroll
    for (int j = 0; j < 4; ++j) { const float lo = (n & 4) ? sc[1][j] : sc[0][j], hi = (n & 4) ? sc[3][j] : sc[2][j]; v[j] = (n & 8) ? hi : lo; }
#pragma unroll
    for (int j = 0; j < 4; j += 2) {
      u32 t = pk(v[j], v[j + 1]);
      t = pk(blo(t) * 0.08838834764831845f, bhi(t) * 0.08838834764831845f);
      v[j] = blo(t) * 1.4426950408889634f; v[j + 1] = bhi(t) * 1.4426950408889634f;
    }
    float m = fmaxf(fmaxf(v[0], v[1]), fmaxf(v[2], v[3]));
    m = fmaxf(m, dpp<ROR4>(m)); m = fmaxf(m, dpp<ROR8>(m)); m = x16_max(m); m = x32_max(m);
#pragma unroll
    for (int j = 0; j < 4; ++j) v[j] = ex2(v[j] - m);
    sts<u32x2>(pwa, u32x2{pk(v[0], v[1]), pk(v[2], v[3])});
    arrive<C_P>(vlp, one);
    float l = (v[0] + v[1]) + (v[2] + v[3]);
    l += dpp<ROR4>(l); l += dpp<ROR8>(l); l = x16_sum(l); l = x32_sum(l);
    sts<float>(L0 + L_M + p * 16 + kw * 4, m);
    sts<float>(L0 + L_L + p * 16 + kw * 4, l);
    arrive<C_M>(vl0, one);
    asm volatile("" : "+v"(f0) :: "memory");
    RETRY(BAD(unseal(f0, tb)), f0 = ld<AUX_SC1>(rw, fo))
    {
      f32x4 a = asf(f0);
#pragma unroll
      for (int j = 0; j < 4; ++j) { a[j] += dpp<ROR8>(a[j]); a[j] = x16_sum(a[j]); }
      const u32 u0 = pk(a[0], a[1]), u1 = pk(a[2], a[3]);
      auto s0 = __builtin_amdgcn_permlane32_swap(u0, u0, false, false), s1 = __builtin_amdgcn_permlane32_swap(u1, u1, false, false);
      const u32 g0 = pk(blo(s0[0]) * sigm(blo(s0[0])), bhi(s0[0]) * sigm(bhi(s0[0]))), g1 = pk(blo(s1[0]) * sigm(blo(s1[0])), bhi(s1[0]) * sigm(bhi(s1[0])));
      sts<u32x2>(L0 + L_H + kw * HS + (lane & 7) * 8, u32x2{pk(blo(g0) * blo(s0[1]), bhi(g0) * bhi(s0[1])), pk(blo(g1) * blo(s1[1]), bhi(g1) * bhi(s1[1]))});
    }
    waitn<C_M, 4, 1>(vl0);
    f32x4 al; u32x4 mu;
    {
      const int hs = lane < 32 ? kw : p;
      const f32x4 mm = lds<f32x4>(L0 + L_M + hs * 16), ll = lds<f32x4>(L0 + L_L + hs * 16);
      const float M = fmaxf(fmaxf(mm[0], mm[1]), fmaxf(mm[2], mm[3]));
#pragma unroll
      for (int j = 0; j < 4; ++j) al[j] = ex2(mm[j] - M);
      mu = u32x4{F2U(M), tag, F2U((al[0] * ll[0] + al[1] * ll[1]) + (al[2] * ll[2] + al[3] * ll[3])), ~tag};
    }
    u32 keep = lane < 32 ? 0xffffff00u : 0u;
    if (lane < 32) mu = tb;
    int pa = lane < 32 ? kvh * RG + WS_PB + ((((kw * 2 + (lane >> 4)) * 4 + b) * 8 + s) * 16 + n) * 16 : (kw == 1 && lane < 36) ? kvh * RG + WS_MB + (r * 4 + p) * 16 : WS_END;
    u32 pr = L0 + L_PO + kw * 512 + (lane & 31) * 16;
    int po = kvh * RG + WS_PB + (((r >> 2) * 4 + kw) * 128 + lane) * 16;
    u32 ow = L0 + L_O + (g * 4 + kw) * OS + n * 8;
    u64 inc = 1;
    GAS u64* a3 = (GAS u64*)(ws + c3_at(kw == 0 ? (((w >> 3) & 3) * 4 + (lane & 3)) * 4 + (w & 3) : (nb * 4 + (kvh >> 1)) * 4 + (lane & 3)));
    const int cg = 4 * w + kw;
    int oo = (cg & 7) * RG + WS_OB + ((cg >> 3) * 256 + lane) * 16;
    int oe = n < 4 ? (p * 512 + 4 * cg + g) * 2 : 4096;
    u64 want = ((u64)tag << 32) | 16u;
    PIN("+v"(pa), "+v"(pr), "+v"(po), "+v"(mu), "+v"(keep), "+v"(ow), "+v"(inc), "+v"(al), "+v"(a3), "+v"(oo), "+v"(oe), "+v"(want), "+v"(tb));
    __builtin_amdgcn_s_setprio(3);
    arrive<C_V>(vl0, one);
    BAR();
    {
      const f32x4 o = (al[0] * lds<f32x4>(pr) + al[1] * lds<f32x4>(pr + 2048)) + (al[2] * lds<f32x4>(pr + 4096) + al[3] * lds<f32x4>(pr + 6144));
      st16(rw, pa, u32x4{(F2U(o[0]) & keep) | mu[0], (F2U(o[1]) & keep) | mu[1], (F2U(o[2]) & keep) | mu[2], (F2U(o[3]) & keep) | mu[3]});
    }
    waitn<C_GO, 1, 0>(vl0);
    u32x4 p0 = ld<AUX_SC1>(rw, po), p1 = ld<AUX_SC1>(rw, po + 1024);
    RETRY(BAD(unseal(p0, tb) | unseal(p1, tb)), (p0 = ld<AUX_SC1>(rw, po), p1 = ld<AUX_SC1>(rw, po + 1024)))
    sts<u32x2>(ow, u32x2{pk(U2F(p0[0]), U2F(p0[1])), pk(U2F(p0[2]), U2F(p0[3]))});
    sts<u32x2>(ow + 16 * OS, u32x2{pk(U2F(p1[0]), U2F(p1[1])), pk(U2F(p1[2]), U2F(p1[3]))});
    BAR();
    if (kw == 1) count_up<15>(a3, inc);
    if (w < 32) {
      u32 cnt = HINT_MAX;
      if (kw == 0) { poll_first<15>(a3); poll<15, 6>(a3, want, 0, cnt); arrive<C_GO>(vl0, one); } else waitn<C_GO, 2, 0>(vl0);
      u32x4 e0 = ld<AUX_SC1>(rw, oo), e1 = ld<AUX_SC1>(rw, oo + 1024), e2 = ld<AUX_SC1>(rw, oo + 2048), e3 = ld<AUX_SC1>(rw, oo + 3072);
      RETRY(BAD(unseal(e0, tb) | unseal(e1, tb) | unseal(e2, tb) | unseal(e3, tb)),
            (e0 = ld<AUX_SC1>(rw, oo), e1 = ld<AUX_SC1>(rw, oo + 1024), e2 = ld<AUX_SC1>(rw, oo + 2048), e3 = ld<AUX_SC1>(rw, oo + 3072)))
      const f32x4 e = (asf(e0) + asf(e1)) + (asf(e2) + asf(e3));
      auto s0 = __builtin_amdgcn_permlane32_swap(F2U(e[0]), F2U(e[2]), false, false), s1 = __builtin_amdgcn_permlane32_swap(F2U(e[1]), F2U(e[3]), false, false);
      auto s2 = __builtin_amdgcn_permlane16_swap(F2U(U2F(s0[0]) + U2F(s0[1])), F2U(U2F(s1[0]) + U2F(s1[1])), false, false);
      float f = U2F(s2[0]) + U2F(s2[1]);
      f += dpp<ROR8>(f); f += dpp<ROR4>(f);
      __builtin_amdgcn_raw_buffer_store_b16((u16)pk(f, f), mk(out, 4096), oe, 0, AUX_SC1);
      if (cg == 0) __builtin_amdgcn_raw_buffer_store_b32(tag, rw, lane == 0 ? 0 : WS_END, 0, AUX_SC1);
    }
    return;
  }
  int mo = kvh * RG + WS_MB + ((p * 8 + q4) * 4 + (r >> 3)) * 16;
  if (wv < 4) {
    tag = get_tag(ws);
    const u32x4 tb = tag_bytes(tag);
    u32 wp = L0 + L_G1 + (8 * g + q4) * 128 + (((2 * wv + (p >> 1)) ^ ((((q4 >> 1) & 1) | ((g & 1) << 1)) << 1)) << 4) + 8 * (p & 1);
    u32 xp = L0 + L_X + p * XS + kq * 256 + g * 16;
    int dq = n >= 4 ? WS_END : kvh * RG + WS_QB + ((p * 4 + kq) * 128 + cq * 16 + 4 * wv + g) * 16;
    int df = n >= 4 ? WS_END : ((tau >> 3) & 7) * RG + WS_FB + ((((tau >> 6) * 8 + (tau & 7)) * 4 + p) * 16 + 4 * wv + g) * 16;
    PIN("+v"(wp), "+v"(xp), "+v"(dq), "+v"(df));
    if (has_f) asm volatile("s_waitcnt vmcnt(4)" ::: "memory"); else asm volatile("s_waitcnt vmcnt(0)" ::: "memory");
    arrive<C_G1>(vl0, one);
    waitn<C_G1, 4, 0>(vl0);
    u32x4 xf[4];
    {
      u32x4 wf[4];
#pragma unroll
      for (int ks = 0; ks < 4; ++ks) { wf[ks] = trfrag(wp + ks * 4096, 512); xf[ks] = lds<u32x4>(xp + ks * 64); }
      __builtin_amdgcn_sched_barrier(0);
      f32x4 acc = mfma(wf[0], xf[0], f32x4{0.f, 0.f, 0.f, 0.f}), acd = mfma(wf[1], xf[1], f32x4{0.f, 0.f, 0.f, 0.f});
      acc = mfma(wf[2], xf[2], acc); acd = mfma(wf[3], xf[3], acd);
      st16(rw, dq, seal_rn(acc + acd, tb));
    }
    if (has_f) {
      asm volatile("s_waitcnt vmcnt(1)" ::: "memory");
      arrive<C_F1>(vl0, one);
      waitn<C_F1, 4, 0>(vl0);
      u32x4 wf[4];
#pragma unroll
      for (int ks = 0; ks < 4; ++ks) wf[ks] = trfrag(wp + 16384 + ks * 4096, 512);
      __builtin_amdgcn_sched_barrier(0);
      f32x4 acc = mfma(wf[0], xf[0], f32x4{0.f, 0.f, 0.f, 0.f}), acd = mfma(wf[1], xf[1], f32x4{0.f, 0.f, 0.f, 0.f});
      acc = mfma(wf[2], xf[2], acc); acd = mfma(wf[3], xf[3], acd);
      st16(rw, df, seal_rn(acc + acd, tb));
    }
    if (wv == 0 && r < 8)
      __builtin_amdgcn_raw_buffer_store_b64(u32x2{0u, tag}, rw, lane == 0 ? r * RG + WS_C2 + kvh * 256 : lane == 1 ? c3_at(w) : WS_END, 0, AUX_SC1);
    {
      const int qo = kvh * RG + WS_QB + ((b * 4 + (lane >> 5)) * 128 + wv * 32 + (lane & 31)) * 16;
      gate((u32)__builtin_amdgcn_s_memrealtime() + DQ);
      gate(t0 + T1);
      u32x4 q0 = ld<AUX_SC1>(rw, qo), q1 = ld<AUX_SC1>(rw, qo + 4096);
      u32x4 gwv = ld<0>(mk(gw, 1024), lane * 16);
      RETRY(BAD(unseal(q0, tb) | unseal(q1, tb)), (q0 = ld<AUX_SC1>(rw, qo), q1 = ld<AUX_SC1>(rw, qo + 4096)))
      {
        const f32x4 qs = asf(q0) + asf(q1);
        auto s0 = __builtin_amdgcn_permlane32_swap(F2U(qs[0]), F2U(qs[2]), false, false), s1 = __builtin_amdgcn_permlane32_swap(F2U(qs[1]), F2U(qs[3]), false, false);
        sts<u32>(L0 + L_Q + wv * QS + (lane & 31) * 8 + (lane >> 5) * 4, pk(U2F(s0[0]) + U2F(s0[1]), U2F(s1[0]) + U2F(s1[1])));
      }
      arrive<C_Q>(vl0, one);
      PIN("+v"(gwv));
      const u32x4 xs = lds<u32x4>(L0 + L_X + wv * XS + lane * 16);
      float z = 0.f;
#pragma unroll
      for (int j = 0; j < 4; ++j) { const u32 t = pk(blo(xs[j]) * blo(gwv[j]), bhi(xs[j]) * bhi(gwv[j])); z += blo(t); z += bhi(t); }
      z += dpp<XOR1>(z); z += dpp<XOR2>(z); z += dpp<ROR4>(z); z += dpp<ROR8>(z); z = x16_sum(z); z = x32_sum(z);
      sts<float>(L0 + L_G + wv * 4, rbf(sigm(rbf(z))));
    }
    __builtin_amdgcn_s_setprio(0);
    {
      const u32 c1 = ((lane & 15) ^ (g << 1)) << 4;
      const u32 o0 = (kb * 64 + 16 * wv + g) * 1024 + nb * 256, o2 = (beta * 32 + 8 * wv + g) * 1024 + nb * 256 + (c1 ^ ((wv & 1) << 7));
      gate(t0 + G2T + wv * G2S);
      asm volatile("s_mov_b32 m0, %4\n\t" DMA4("s[80:81]")
                   :: "v"(o0 + c1), "v"(o0 + 4096 + c1), "v"(o0 + 8192 + (c1 ^ 128)), "v"(o0 + 12288 + (c1 ^ 128)), "s"(L0 + L_G2 + 4096 * wv) : "memory", "scc");
      if (has_w2)
        asm volatile("s_mov_b32 m0, %2\n\ts_nop 0\n\tglobal_load_lds_dwordx4 %0, s[82:83] nt\n\ts_add_u32 m0, m0, 0x400\n\ts_nop 0\n\tglobal_load_lds_dwordx4 %1, s[82:83] nt"
                     :: "v"(o2), "v"(o2 + 4096), "s"(L0 + L_G2 + 16384 + 2048 * wv) : "memory", "scc");
    }
    __builtin_amdgcn_s_setprio(2);
    arrive<C_V>(vl0, one);
    BAR();
    asm volatile("s_waitcnt vmcnt(0)" ::: "memory");
    arrive<C_G2>(vl0, one);
  } else {
    const int vw = wv - 8;
    __builtin_amdgcn_s_setprio(1);
    const rsrc_t rv = mk((const unsigned char*)vc + kvoff, 65536);
    BAR0();
    tag = get_tag(ws);
    u32x4 vf[4][4];
#pragma unroll
    for (int t = 0; t < 4; ++t) {
      const int tile = ((4 * t + wv - 4 + r) & 15) * 4096;
      gate(t0 + P0 + (4 + t) * PACE + vw * (PACE / 4));
#pragma unroll
      for (int i = 0; i < 4; ++i) vf[t][i] = ld<AUX_NT>(rv, tile + lane * 16 + i * 1024);
      __builtin_amdgcn_sched_barrier(0);
    }
    const int hn = (n + q4) & 3;
    u32 pra = L0 + L_P + vw * 512 + hn * 128 + g * 32, vlp = L0 + vw * 4;
    u32 pw = L0 + L_PO + vw * 2048 + g * 512 + n * 32;
    GAS u64* ap = (GAS u64*)(ws + (lane & 7) * RG + WS_C2 + kvh * 256);
    GAS u64* cp = (GAS u64*)(ws + (r & 7) * RG + WS_C2 + kvh * 256);
    u64 want = ((u64)tag << 32) | 32u, inc = 1;
    f32x4 acc[8];
    u32x4 va[8], vb[8];
#define PERM(t, i, c) __builtin_amdgcn_perm(vf[t][(i) + 1][(c) >> 1], vf[t][i][(c) >> 1], ((c) & 1) ? 0x07060302u : 0x05040100u)
#pragma unroll
    for (int c = 0; c < 8; ++c) va[c] = u32x4{PERM(0, 0, c), PERM(0, 2, c), PERM(1, 0, c), PERM(1, 2, c)};
    PIN("+v"(va[0]), "+v"(va[1]), "+v"(va[2]), "+v"(va[3]), "+v"(va[4]), "+v"(va[5]), "+v"(va[6]), "+v"(va[7]), "+v"(pra), "+v"(vlp), "+v"(pw));
    PIN("+v"(ap), "+v"(cp), "+v"(want), "+v"(inc), "+v"(mo));
    __builtin_amdgcn_sched_barrier(0);
#pragma unroll
    for (int c = 0; c < 8; ++c) { vb[c][0] = PERM(2, 0, c); vb[c][1] = PERM(2, 2, c); vb[c][2] = vb[c][0]; vb[c][3] = vb[c][0]; }
    PIN("+v"(vb[0]), "+v"(vb[1]), "+v"(vb[2]), "+v"(vb[3]), "+v"(vb[4]), "+v"(vb[5]), "+v"(vb[6]), "+v"(vb[7]));
    __builtin_amdgcn_s_setprio(3);
    waitn<C_P, 1, 1>(vlp);
    u32x4 pf0 = lds<u32x4>(pra), pf1 = lds<u32x4>(pra + 16);
#pragma unroll
    for (int c = 0; c < 8; ++c) acc[c] = mfma(pf0, va[c], f32x4{0.f, 0.f, 0.f, 0.f});
    __builtin_amdgcn_sched_barrier(0);
#pragma unroll
    for (int c = 0; c < 8; ++c) vb[c][2] = PERM(3, 0, c);
    PIN("+v"(vb[0]), "+v"(vb[1]), "+v"(vb[2]), "+v"(vb[3]), "+v"(vb[4]), "+v"(vb[5]), "+v"(vb[6]), "+v"(vb[7]), "+v"(pf1));
    __builtin_amdgcn_sched_barrier(0);
    PIN("+v"(vf[3][2]), "+v"(vf[3][3]));
    arrive<C_V>(vl0, one);
#pragma unroll
    for (int c = 0; c < 8; ++c) { vb[c][3] = PERM(3, 2, c); acc[c] = mfma(pf1, vb[c], acc[c]); }
    sts<f32x4>(pw, f32x4{acc[0][0], acc[1][0], acc[2][0], acc[3][0]});
    sts<f32x4>(pw + 16, f32x4{acc[4][0], acc[5][0], acc[6][0], acc[7][0]});
    if (vw == 0) {
      waitn<C_V, 12, 0>(vl0);
      count_up<255>(ap, inc);
      poll_first<1>(cp);
      BAR();
      u32 cnt = HINT_MAX;
      poll<1, 8>(cp, want, 20, cnt);
      arrive<C_GO>(vl0, one);
      asm volatile("global_load_dwordx4 v[152:155], %0, %1 sc1\n\tglobal_load_dwordx4 v[156:159], %0, %1 offset:256 sc1" :: "v"(mo), "s"(ws)
                   : "memory", "v152", "v153", "v154", "v155", "v156", "v157", "v158", "v159");
    } else {
      BAR();
    }
    __builtin_amdgcn_s_setprio(2);
  }
  const int ct = wv < 4 ? wv : wv - 4;
  u32 wa0 = L0 + L_G2 + (8 * g + q4) * 256 + (((2 * ct + (p >> 1)) ^ ((q4 | ((g & 1) << 2)) << 1)) << 4) + 8 * (p & 1);
  u32 orr = L0 + L_O + n * OS + g * 16, sa = L0 + L_S + n * 8;
  int oa = n >= 4 ? WS_END : ((4 * ct + g) & 7) * RG + WS_OB + (((4 * nb + (ct >> 1)) * 64 + kb) * 4 + p) * 16;
  float gt = lds<float>(L0 + L_G + p * 4);
  const u32x4 hf = lds<u32x4>(L0 + L_H + p * HS + g * 16);
  const float og = n < 4 ? rbf(1.f - gt) : 0.f;
  u32x4 tb = tag_bytes(tag);
  waitn<C_G2, 4, 0>(vl0);
  f32x4 ab = {0.f, 0.f, 0.f, 0.f};
  if (has_w2) ab = mfma(trfrag(wa0 + 16384, 1024), hf, ab) * og;
  u32x4 wf0 = trfrag(wa0, 1024), wf1 = trfrag(wa0 + 8192, 1024);
  PIN("+v"(wf0), "+v"(wf1), "+v"(ab), "+v"(orr), "+v"(oa), "+v"(sa), "+v"(gt), "+v"(tb));
  if (wv == 8) {
    float ma, mb, la, lb; u32 bad;
    asm volatile("s_waitcnt vmcnt(0)\n\tv_xor_b32 %4, %5, v153\n\tv_mov_b32 %0, v152\n\tv_bitop3_b32 %4, %5, v157, %4 bitop3:0xbe\n\tv_mov_b32 %1, v156\n\t"
                 "v_bitop3_b32 %4, %6, v155, %4 bitop3:0xbe\n\tv_mov_b32 %2, v154\n\tv_bitop3_b32 %4, %6, v159, %4 bitop3:0xbe\n\tv_mov_b32 %3, v158"
                 : "=&v"(ma), "=&v"(mb), "=&v"(la), "=&v"(lb), "=&v"(bad) : "s"(tag), "s"(~tag) : "memory");
    if (__builtin_expect(__any(bad != 0), 0)) {
      _Pragma("nounroll") for (int sp = 0; sp < SPIN_MAX; ++sp) {
        const u32x4 m0 = ld<AUX_SC1>(rw, mo), m1 = ld<AUX_SC1>(rw, mo + 256);
        asm volatile("" ::: "memory");
        ma = U2F(m0[0]); mb = U2F(m1[0]); la = U2F(m0[2]); lb = U2F(m1[2]);
        if (!__any(((m0[1] ^ tag) | (m0[3] ^ ~tag) | (m1[1] ^ tag) | (m1[3] ^ ~tag)) != 0)) break;
      }
    }
    float M;
    asm volatile("v_max_f32 %0, %1, %2\n\ts_nop 1\n\tv_max_f32_dpp %0, %0, %0 row_ror:4 row_mask:0xf bank_mask:0xf\n\ts_nop 1\n\tv_max_f32_dpp %0, %0, %0 row_ror:8 row_mask:0xf bank_mask:0xf"
                 : "=&v"(M) : "v"(ma), "v"(mb));
    const float e0 = ex2(ma - M), e1 = ex2(mb - M);
    float Ls = e0 * la + e1 * lb; Ls += dpp<ROR4>(Ls); Ls += dpp<ROR8>(Ls);
    const float sc = gt * rcp(Ls);
    sts<f32x2>(sa, f32x2{e0 * sc, e1 * sc});
  }
  BAR();
  {
    const u32x4 a0 = lds<u32x4>(orr), a1 = lds<u32x4>(orr + 16 * OS), a2 = lds<u32x4>(orr + 64), a3 = lds<u32x4>(orr + 16 * OS + 64);
    const f32x2 sw = lds<f32x2>(sa);
    __builtin_amdgcn_sched_barrier(0);
    f32x4 d0 = mfma(wf0, a0, f32x4{0.f, 0.f, 0.f, 0.f}), d1 = mfma(wf0, a1, f32x4{0.f, 0.f, 0.f, 0.f});
    __builtin_amdgcn_sched_barrier(0);
    d0 = mfma(wf1, a2, d0); d1 = mfma(wf1, a3, d1);
    ab += sw[0] * d0; ab += sw[1] * d1;
  }
#pragma unroll
  for (int j = 0; j < 4; ++j) ab[j] += dpp<ROR4>(ab[j]);
#pragma unroll
  for (int j = 0; j < 4; ++j) ab[j] += dpp<ROR8>(ab[j]);
  st16(rw, oa, seal(ab, tb));
}

extern "C" void kernel_launch(void* const* d_in, const int* in_sizes, int n_in, void* d_out, int out_size, void* d_ws, size_t ws_size, hipStream_t stream) {
  fused_decode<<<256, 768, 0, stream>>>((const u16*)d_in[3], (const u16*)d_in[6], (const u16*)d_in[7], (const u16*)d_in[1], (const u16*)d_in[2], (const u16*)d_in[0],
      (unsigned char*)d_ws, (const u16*)d_in[9], (const u16*)d_in[4], (const u16*)d_in[8], (u16*)d_out);
}
```

```cpp
#include <hip/hip_runtime.h>
#include <stdint.h>

typedef unsigned short u16;
typedef unsigned u32;
typedef unsigned long long u64;
typedef u32 u32x4 __attribute__((ext_vector_type(4)));
typedef u32 u32x2 __attribute__((ext_vector_type(2)));
typedef float f32x4 __attribute__((ext_vector_type(4)));
typedef float f32x2 __attribute__((ext_vector_type(2)));
typedef short s16x4 __attribute__((ext_vector_type(4)));
typedef __bf16 bf16x8 __attribute__((ext_vector_type(8)));
typedef __bf16 bf16x2 __attribute__((ext_vector_type(2)));
#define LAS __attribute__((address_space(3)))
#define GAS __attribute__((address_space(1)))
typedef __amdgpu_buffer_rsrc_t rsrc_t;

#define OAUX 0
#define EPE 1
#define WIDE 0
constexpr int SK = 8192, WIN = 2048;
constexpr int PACE = 58;
constexpr int P0 = 80;
constexpr int G2T = P0 + 8 * PACE + 10, G2S = 3 * PACE / 8;
constexpr int T1 = 300, DQ = 90;
constexpr int RG = 266496;
constexpr int WS_QB = 4096, WS_FB = WS_QB + 32768, WS_MB = WS_FB + 32768, WS_C2 = WS_MB + 2048, WS_C3 = WS_C2 + 2048, WS_PB = WS_C3 + 2048,
              WS_OB = WS_PB + 65536, WS_END = 8 * RG;
static_assert(WS_OB + 65536 <= RG, "region map");
constexpr int L_PO = 0, L_P = 8192, L_X = L_P + 2048, XS = 1040, L_Q = L_X + 4 * XS, QS = 272, L_M = L_Q + 4 * QS, L_L = L_M + 64,
              L_O = L_L + 64, OS = 144, L_S = L_O + 32 * OS, L_H = L_S + 128, HS = 80, L_G = L_H + 4 * HS, L_CNT = L_G + 16,
              L_G2 = (L_CNT + 64 + 255) & ~255, L_G1 = L_G2 + 24576, L_K = L_G1 + 32768, L_END = L_K + 65536;
constexpr int C_G1 = L_CNT, C_Q = L_CNT + 4, C_M = L_CNT + 8, C_V = L_CNT + 12, C_G2 = L_CNT + 16, C_GO = L_CNT + 20, C_F1 = L_CNT + 24,
              C_T0 = L_CNT + 28, C_P = L_CNT + 32;
constexpr int AUX_NT = 2, AUX_SC1 = 16;
constexpr int SPIN_MAX = 1 << 20, HINT_MAX = 512;

#define PIN(...) asm volatile("" : __VA_ARGS__)

__device__ __forceinline__ int c3_at(int j) { return (j & 7) * RG + WS_C3 + (j >> 3) * 256; }
__device__ __forceinline__ void gate(u32 until) {
  asm volatile("1:\n\ts_memrealtime vcc\n\ts_waitcnt lgkmcnt(0)\n\ts_sub_i32 vcc_lo, vcc_lo, %0\n\ts_cmp_ge_i32 vcc_lo, 0\n\ts_cbranch_scc1 2f\n\ts_sleep 1\n\ts_branch 1b\n2:"
               :: "s"(until) : "memory", "vcc", "scc");
}
__device__ __forceinline__ rsrc_t mk(const void* p, u32 n) { return __builtin_amdgcn_make_buffer_rsrc((void*)p, 0, n, 0x00020000); }
template <int AUX> __device__ __forceinline__ u32x4 ld(rsrc_t r, int off) { return __builtin_amdgcn_raw_buffer_load_b128(r, off, 0, AUX); }
__device__ __forceinline__ void st16(rsrc_t r, int off, u32x4 v) { __builtin_amdgcn_raw_buffer_store_b128(v, r, off, 0, AUX_SC1); }
#define PISSUE "global_atomic_add_x2 v[160:161], %[p], %[z], off sc0 sc1\n\t"
#define PCHECK "s_sleep 1\n\tv_cmp_ne_u64 vcc, v[160:161], %[want]\n\ts_cbranch_vccz 2f\n\t"
template <int MASK> __device__ __forceinline__ void poll_first(GAS u64* p) {
  u64 sv; u64 zero = 0;
  asm volatile("s_mov_b64 %[sv], exec\n\ts_mov_b64 exec, %[mask]\n\tv_mov_b64 v[160:161], -1\n\t" PISSUE "s_mov_b64 exec, %[sv]"
               : [sv] "=&s"(sv) : [p] "v"(p), [z] "v"(zero), [mask] "i"(MASK) : "memory", "v160", "v161");
}
template <int MASK, int DEPTH> __device__ __forceinline__ void poll(GAS u64* p, u64 want, u32 thr, u32& cnt) {
  u64 sv; u64 zero = 0;
  asm volatile("s_mov_b64 %[sv], exec\n\ts_mov_b64 exec, %[mask]\n"
               "1:\n\t" PCHECK PCHECK PCHECK
               "v_cmp_gt_u32 vcc, %[thr], v160\n\ts_cbranch_vccz 3f\n\ts_sleep 6\n"
               "3:\n\ts_waitcnt vmcnt(%[depth])\n\tv_cmp_ne_u64 vcc, v[160:161], %[want]\n\ts_cbranch_vccz 2f\n\t" PISSUE "s_sub_u32 %[cnt], %[cnt], 1\n\ts_cbranch_scc0 1b\n"
               "2:\n\ts_mov_b64 exec, %[sv]"
               : [sv] "=&s"(sv), [cnt] "+s"(cnt) : [p] "v"(p), [z] "v"(zero), [want] "v"(want), [thr] "s"(thr), [mask] "i"(MASK), [depth] "i"(DEPTH - 1)
               : "memory", "vcc", "scc", "v160", "v161");
}
template <int MASK> __device__ __forceinline__ void count_up(GAS u64* p, u64 inc) {
  u64 sv;
  asm volatile("s_mov_b64 %0, exec\n\ts_mov_b64 exec, %3\n\tglobal_atomic_add_x2 %1, %2, off sc1\n\ts_mov_b64 exec, %0" : "=&s"(sv) : "v"(p), "v"(inc), "i"(MASK) : "memory");
}
__device__ __forceinline__ f32x4 mfma(u32x4 a, u32x4 b, f32x4 c) {
  return __builtin_amdgcn_mfma_f32_16x16x32_bf16(__builtin_bit_cast(bf16x8, a), __builtin_bit_cast(bf16x8, b), c, 0, 0, 0);
}
__device__ __forceinline__ u32x4 trfrag(u32 a, int rowbytes4) {
  s16x4 x = __builtin_amdgcn_ds_read_tr16_b64_v4i16((LAS s16x4*)(uintptr_t)a);
  s16x4 y = __builtin_amdgcn_ds_read_tr16_b64_v4i16((LAS s16x4*)(uintptr_t)(a + rowbytes4));
  u32x2 u = __builtin_bit_cast(u32x2, x), v = __builtin_bit_cast(u32x2, y);
  return u32x4{u[0], u[1], v[0], v[1]};
}
template <typename T> __device__ __forceinline__ T lds(u32 a) { return *(LAS T*)(uintptr_t)a; }
template <typename T> __device__ __forceinline__ void sts(u32 a, T v) { *(LAS T*)(uintptr_t)a = v; }
__device__ __forceinline__ float U2F(u32 x) { return __uint_as_float(x); }
__device__ __forceinline__ u32 F2U(float x) { return __float_as_uint(x); }
__device__ __forceinline__ float rbf(float f) { return (float)(__bf16)f; }
__device__ __forceinline__ u32 pk(float lo, float hi) { bf16x2 v = {(__bf16)lo, (__bf16)hi}; return __builtin_bit_cast(u32, v); }
__device__ __forceinline__ float blo(u32 w) { return U2F(w << 16); }
__device__ __forceinline__ float bhi(u32 w) { return U2F(w & 0xffff0000u); }
__device__ __forceinline__ float rcp(float x) { return __builtin_amdgcn_rcpf(x); }
__device__ __forceinline__ float ex2(float x) { return __builtin_amdgcn_exp2f(x); }
__device__ __forceinline__ float sigm(float z) { return rcp(1.f + ex2(-1.4426950408889634f * z)); }
template <int CTRL> __device__ __forceinline__ float dpp(float x) {
  return __builtin_bit_cast(float, __builtin_amdgcn_mov_dpp(__builtin_bit_cast(int, x), CTRL, 0xf, 0xf, true));
}
constexpr int XOR1 = 0xB1, XOR2 = 0x4E, ROR4 = 0x124, ROR8 = 0x128;
__device__ __forceinline__ float x16_sum(float x) { auto s = __builtin_amdgcn_permlane16_swap(F2U(x), F2U(x), false, false); return U2F(s[0]) + U2F(s[1]); }
__device__ __forceinline__ float x32_sum(float x) { auto s = __builtin_amdgcn_permlane32_swap(F2U(x), F2U(x), false, false); return U2F(s[0]) + U2F(s[1]); }
__device__ __forceinline__ float x16_max(float x) { auto s = __builtin_amdgcn_permlane16_swap(F2U(x), F2U(x), false, false); return fmaxf(U2F(s[0]), U2F(s[1])); }
__device__ __forceinline__ float x32_max(float x) { auto s = __builtin_amdgcn_permlane32_swap(F2U(x), F2U(x), false, false); return fmaxf(U2F(s[0]), U2F(s[1])); }
__device__ __forceinline__ u32x4 tag_bytes(u32 tag) { return u32x4{tag & 0xff, (tag >> 8) & 0xff, (tag >> 16) & 0xff, tag >> 24}; }
__device__ __forceinline__ u32x4 seal(f32x4 v, u32x4 tb) {
  return u32x4{(F2U(v[0]) & 0xffffff00u) | tb[0], (F2U(v[1]) & 0xffffff00u) | tb[1], (F2U(v[2]) & 0xffffff00u) | tb[2], (F2U(v[3]) & 0xffffff00u) | tb[3]};
}
__device__ __forceinline__ u32x4 seal_rn(f32x4 v, u32x4 tb) {
  return seal(__builtin_bit_cast(f32x4, __builtin_bit_cast(u32x4, v) + 0x80u), tb);
}
__device__ __forceinline__ u32 unseal(u32x4& v, u32x4 tb) { v ^= tb; return v[0] | v[1] | v[2] | v[3]; }
#define BAD(x) __any(((x) & 0xff) != 0)
__device__ __forceinline__ f32x4 asf(u32x4 v) { return __builtin_bit_cast(f32x4, v); }
__device__ __forceinline__ u32 get_tag(const unsigned char* ws) {
  u32 ep;
  asm volatile("s_load_dword %0, %1, 0x0\n\ts_waitcnt lgkmcnt(0)" : "=s"(ep) : "s"(ws) : "memory");
  return ep + 0x3b5d2f47u;
}
#define BAR() asm volatile("s_waitcnt lgkmcnt(0)\n\ts_barrier" ::: "memory")
#define BAR0() do { BAR(); t0 = __builtin_amdgcn_readfirstlane(lds<u32>(L0 + C_T0)); } while (0)
template <int OFF> __device__ __forceinline__ void arrive(u32 l0, u32 one) {
  u64 sv;
  asm volatile("s_mov_b64 %0, exec\n\ts_mov_b64 exec, 1\n\tds_add_u32 %1, %2 offset:%3\n\ts_mov_b64 exec, %0" : "=&s"(sv) : "v"(l0), "v"(one), "i"(OFF) : "memory");
}
template <int OFF, int N, int SLEEP> __device__ __forceinline__ void waitn(u32 l0) {
  u32 t;
  asm volatile("s_branch 1f\n0:\n\ts_sleep %4\n1:\n\tds_read_b32 %0, %1 offset:%2\n\ts_waitcnt lgkmcnt(0)\n\tv_cmp_ne_u32 vcc, %3, %0\n\ts_cbranch_vccnz 0b"
               : "=&v"(t) : "v"(l0), "i"(OFF), "i"(N), "i"(SLEEP) : "memory", "vcc");
}
#define RETRY(cond, reload) \
  if (__builtin_expect(cond, 0)) { _Pragma("nounroll") for (int sp = 0; sp < SPIN_MAX; ++sp) { reload; asm volatile("" ::: "memory"); if (!(cond)) break; } }

__global__ void __launch_bounds__(768, 3) fused_decode(const u16*, const u16*, const u16*, const u16*, const u16*, const u16*, unsigned char*,
                                                       const u16*, const u16*, const u16*, u16*) {
  asm volatile("s_load_dwordx16 s[64:79], %0, 0x0\n\ts_load_dwordx4 s[80:83], %0, 0x40\n\ts_load_dwordx2 s[84:85], %0, 0x50"
               :: "s"(__builtin_amdgcn_kernarg_segment_ptr())
               : "memory", "s64", "s65", "s66", "s67", "s68", "s69", "s70", "s71", "s72", "s73", "s74", "s75", "s76", "s77", "s78", "s79", "s80", "s81",
                 "s82", "s83", "s84", "s85");
  __shared__ __attribute__((aligned(256))) unsigned char smem[L_END];
  const u32 L0 = (u32)(uintptr_t)(LAS unsigned char*)smem;
  const int w = blockIdx.x, tid = threadIdx.x, lane = tid & 63;
  int wv = __builtin_amdgcn_readfirstlane(tid >> 6);
  const int kvh = w & 7, r = w >> 3, b = r >> 3, s = r & 7;
  const int n = lane & 15, g = lane >> 4, q4 = n >> 2, p = n & 3;
  const int kq = r & 3, tau = kvh * 28 + r, cq = ((r >> 2) + 4) & 7;
  const bool has_f = r < 28;
  const u32 kvoff = (u32)(((b * 8 + kvh) * SK + (SK - WIN) + s * 256) * 256);
  u32 t0 = 0;
#define DMA4(base) "s_nop 0\n\tglobal_load_lds_dwordx4 %0, " base " nt\n\ts_add_u32 m0, m0, 0x400\n\ts_nop 0\n\tglobal_load_lds_dwordx4 %1, " base " nt\n\t" \
                   "s_add_u32 m0, m0, 0x400\n\ts_nop 0\n\tglobal_load_lds_dwordx4 %2, " base " nt\n\ts_add_u32 m0, m0, 0x400\n\ts_nop 0\n\tglobal_load_lds_dwordx4 %3, " base " nt"
  if (wv >= 8) {
  } else if (wv >= 4) {
    __builtin_amdgcn_s_setprio(2);
    const u32 k0 = kvoff + g * 256 + ((n ^ (4 * g)) << 4);
    BAR0();
#pragma unroll
    for (int t = 0; t < 4; ++t) {
      const u32 o = k0 + ((4 * t + wv + r) & 15) * 4096;
      gate(t0 + P0 + t * PACE + (wv - 4) * (PACE / 4));
      asm volatile("s_mov_b32 m0, %4\n\ts_nop 0\n\tglobal_load_lds_dwordx4 %0, s[70:71] nt\n\tglobal_load_lds_dwordx4 %1, s[70:71] offset:1024 nt\n\t"
                   "global_load_lds_dwordx4 %2, s[70:71] offset:2048 nt\n\tglobal_load_lds_dwordx4 %3, s[70:71] offset:3072 nt"
                   :: "v"(o), "v"(o ^ 16), "v"(o ^ 32), "v"(o ^ 48), "s"(L0 + L_K + (wv - 4) * 16384 + t * 4096) : "memory");
    }
  } else {
    const u32 row = 128 * kq + 32 * wv + (lane >> 3), c0 = ((lane & 7) ^ (((lane >> 4) & 1) << 1)) << 4;
    const u32 oq0 = row * 8192 + kvh * 1024 + cq * 128 + c0, oq1 = (oq0 ^ 64) + 8 * 8192;
    u32 of0 = row * 3584 + (tau >> 3) * 128 + c0, of1 = (of0 ^ 64) + 8 * 3584, of2 = of0 + 16 * 3584, of3 = of1 + 16 * 3584;
    u32 mf = L0 + L_G1 + 16384 + 4096 * wv;
    PIN("+v"(of0), "+v"(of1), "+v"(of2), "+v"(of3), "+s"(mf));
    asm volatile("s_mov_b32 m0, %4\n\ts_waitcnt lgkmcnt(0)\n\t" DMA4("s[64:65]") "\n\ts_mov_b32 m0, %6\n\ts_nop 0\n\tglobal_load_lds_dwordx4 %5, s[74:75]"
                 :: "v"(oq0), "v"(oq1), "v"(oq0 + 16 * 8192), "v"(oq1 + 16 * 8192), "s"(L0 + L_G1 + 4096 * wv), "v"(tid * 16), "s"(L0 + L_X + wv * XS) : "memory", "scc");
    if (has_f)
      asm volatile("s_bitcmp1_b32 %5, 2\n\ts_cselect_b64 s[66:67], s[68:69], s[66:67]\n\ts_mov_b32 m0, %4\n\t" DMA4("s[66:67]")
                   :: "v"(of0), "v"(of1), "v"(of2), "v"(of3), "s"(mf), "s"(tau) : "memory", "scc");
    __builtin_amdgcn_s_setprio(3);
    if (tid < 16) sts<u32>(L0 + L_CNT + tid * 4, tid == 7 ? (u32)__builtin_amdgcn_s_memrealtime() : 0u);
    BAR0();
  }
  const u16 *vc, *gw; unsigned char* ws; u16* out;
  asm volatile("s_waitcnt lgkmcnt(0)\n\ts_mov_b64 %0, s[72:73]\n\ts_mov_b64 %1, s[76:77]\n\ts_mov_b64 %2, s[78:79]\n\ts_mov_b64 %3, s[84:85]"
               : "=s"(vc), "=s"(ws), "=s"(gw), "=s"(out) :: "memory");
  asm volatile("" : "+s"(wv));
  u32 tag;
  const rsrc_t rw = mk(ws, WS_END);
  const int nb = r & 3, kb = 8 * kvh + (r >> 2), beta = 7 * kvh + (r >> 2);
  const bool has_w2 = (r >> 2) < 7;
  u32 vl0 = L0, one = 1; PIN("+v"(vl0), "+v"(one));

  if (wv >= 4 && wv < 8) {
    const int kw = wv - 4;
    tag = get_tag(ws);
    u32x4 tb = tag_bytes(tag);
    asm volatile("s_waitcnt vmcnt(0)" ::: "memory");
    const int bt = has_w2 ? beta : 0;
    const int fo = ((bt >> 1) & 7) * RG + WS_FB + ((((bt >> 4) * 8 + (lane >> 3)) * 4 + kw) * 16 + (bt & 1) * 8 + (lane & 7)) * 16;
    u32x4 f0 = ld<AUX_SC1>(rw, fo);
    u32x4 kf[4][4];
    {
      const u32 ka = L0 + L_K + kw * 16384 + (4 * p + q4) * 256 + ((g ^ n) << 4);
#pragma unroll
      for (int i = 0; i < 4; ++i)
#pragma unroll
        for (int t = 0; t < 4; ++t) kf[t][i] = lds<u32x4>((ka ^ (i << 6)) + t * 4096);
    }
    u32 qa = L0 + L_Q + p * QS + g * 16, pwa = L0 + L_P + kw * 512 + p * 128 + g * 32 + q4 * 8, vlp = L0 + kw * 4;
#pragma unroll
    for (int t = 0; t < 4; ++t) PIN("+v"(kf[t][0]), "+v"(kf[t][1]), "+v"(kf[t][2]), "+v"(kf[t][3]));
    PIN("+v"(qa), "+v"(pwa), "+v"(vlp));
    waitn<C_Q, 4, 1>(vl0);
    u32x4 qf[4];
#pragma unroll
    for (int i = 0; i < 4; ++i) qf[i] = lds<u32x4>(qa + i * 64);
    f32x4 sc[4];
#pragma unroll
    for (int t = 0; t < 4; ++t) sc[t] = mfma(kf[t][0], qf[0], f32x4{0.f, 0.f, 0.f, 0.f});
#pragma unroll
    for (int i = 1; i < 4; ++i)
#pragma unroll
      for (int t = 0; t < 4; ++t) sc[t] = mfma(kf[t][i], qf[i], sc[t]);
    float v[4];
#pragma unroll
    for (int j = 0; j < 4; ++j) { const float lo = (n & 4) ? sc[1][j] : sc[0][j], hi = (n & 4) ? sc[3][j] : sc[2][j]; v[j] = (n & 8) ? hi : lo; }
#pragma unroll
    for (int j = 0; j < 4; j += 2) {
      u32 t = pk(v[j], v[j + 1]);
      t = pk(blo(t) * 0.08838834764831845f, bhi(t) * 0.08838834764831845f);
      v[j] = blo(t) * 1.4426950408889634f; v[j + 1] = bhi(t) * 1.4426950408889634f;
    }
    float m = fmaxf(fmaxf(v[0], v[1]), fmaxf(v[2], v[3]));
    m = fmaxf(m, dpp<ROR4>(m)); m = fmaxf(m, dpp<ROR8>(m)); m = x16_max(m); m = x32_max(m);
#pragma unroll
    for (int j = 0; j < 4; ++j) v[j] = ex2(v[j] - m);
    sts<u32x2>(pwa, u32x2{pk(v[0], v[1]), pk(v[2], v[3])});
    arrive<C_P>(vlp, one);
    float l = (v[0] + v[1]) + (v[2] + v[3]);
    l += dpp<ROR4>(l); l += dpp<ROR8>(l); l = x16_sum(l); l = x32_sum(l);
    sts<float>(L0 + L_M + p * 16 + kw * 4, m);
    sts<float>(L0 + L_L + p * 16 + kw * 4, l);
    arrive<C_M>(vl0, one);
    asm volatile("" : "+v"(f0) :: "memory");
    RETRY(BAD(unseal(f0, tb)), f0 = ld<AUX_SC1>(rw, fo))
    {
      f32x4 a = asf(f0);
#pragma unroll
      for (int j = 0; j < 4; ++j) { a[j] += dpp<ROR8>(a[j]); a[j] = x16_sum(a[j]); }
      const u32 u0 = pk(a[0], a[1]), u1 = pk(a[2], a[3]);
      auto s0 = __builtin_amdgcn_permlane32_swap(u0, u0, false, false), s1 = __builtin_amdgcn_permlane32_swap(u1, u1, false, false);
      const u32 g0 = pk(blo(s0[0]) * sigm(blo(s0[0])), bhi(s0[0]) * sigm(bhi(s0[0]))), g1 = pk(blo(s1[0]) * sigm(blo(s1[0])), bhi(s1[0]) * sigm(bhi(s1[0])));
      sts<u32x2>(L0 + L_H + kw * HS + (lane & 7) * 8, u32x2{pk(blo(g0) * blo(s0[1]), bhi(g0) * bhi(s0[1])), pk(blo(g1) * blo(s1[1]), bhi(g1) * bhi(s1[1]))});
    }
    waitn<C_M, 4, 1>(vl0);
    f32x4 al; u32x4 mu;
    {
      const int hs = lane < 32 ? kw : p;
      const f32x4 mm = lds<f32x4>(L0 + L_M + hs * 16), ll = lds<f32x4>(L0 + L_L + hs * 16);
      const float M = fmaxf(fmaxf(mm[0], mm[1]), fmaxf(mm[2], mm[3]));
#pragma unroll
      for (int j = 0; j < 4; ++j) al[j] = ex2(mm[j] - M);
      mu = u32x4{F2U(M), tag, F2U((al[0] * ll[0] + al[1] * ll[1]) + (al[2] * ll[2] + al[3] * ll[3])), ~tag};
    }
    u32 keep = lane < 32 ? 0xffffff00u : 0u;
    if (lane < 32) mu = tb;
    int pa = lane < 32 ? kvh * RG + WS_PB + ((((kw * 2 + (lane >> 4)) * 4 + b) * 8 + s) * 16 + n) * 16 : (kw == 1 && lane < 36) ? kvh * RG + WS_MB + (r * 4 + p) * 16 : WS_END;
    u32 pr = L0 + L_PO + kw * 512 + (lane & 31) * 16;
    int po = kvh * RG + WS_PB + (((r >> 2) * 4 + kw) * 128 + lane) * 16;
    u32 ow = L0 + L_O + (g * 4 + kw) * OS + n * 8;
    u64 inc = 1;
    GAS u64* a3 = (GAS u64*)(ws + c3_at(kw == 0 ? ((kvh >> 1) * 4 + (lane & 3)) * 4 + (r & 3) : (nb * 4 + (kvh >> 1)) * 4 + (lane & 3)));
    const int cg = 4 * w + kw;
    int oo = (cg & 7) * RG + WS_OB + ((cg >> 3) * 256 + lane) * 16;
    int oe = WIDE ? ((lane & 28) == 0 ? cg * 32 + p * 8 + (lane >> 5) * 4 : 4096) : n < 4 ? (cg * 16 + 4 * p + g) * 2 : 4096;
    u64 want = ((u64)tag << 32) | 16u;
    PIN("+v"(pa), "+v"(pr), "+v"(po), "+v"(mu), "+v"(keep), "+v"(ow), "+v"(inc), "+v"(al), "+v"(a3), "+v"(oo), "+v"(oe), "+v"(want), "+v"(tb));
    __builtin_amdgcn_s_setprio(3);
    arrive<C_V>(vl0, one);
    BAR();
    {
      const f32x4 o = (al[0] * lds<f32x4>(pr) + al[1] * lds<f32x4>(pr + 2048)) + (al[2] * lds<f32x4>(pr + 4096) + al[3] * lds<f32x4>(pr + 6144));
      st16(rw, pa, u32x4{(F2U(o[0]) & keep) | mu[0], (F2U(o[1]) & keep) | mu[1], (F2U(o[2]) & keep) | mu[2], (F2U(o[3]) & keep) | mu[3]});
    }
    waitn<C_GO, 1, 0>(vl0);
    u32x4 p0 = ld<AUX_SC1>(rw, po), p1 = ld<AUX_SC1>(rw, po + 1024);
    RETRY(BAD(unseal(p0, tb) | unseal(p1, tb)), (p0 = ld<AUX_SC1>(rw, po), p1 = ld<AUX_SC1>(rw, po + 1024)))
    sts<u32x2>(ow, u32x2{pk(U2F(p0[0]), U2F(p0[1])), pk(U2F(p0[2]), U2F(p0[3]))});
    sts<u32x2>(ow + 16 * OS, u32x2{pk(U2F(p1[0]), U2F(p1[1])), pk(U2F(p1[2]), U2F(p1[3]))});
    BAR();
    if (kw == 1) count_up<15>(a3, inc);
    if (w < 32) {
      u32 cnt = HINT_MAX;
      if (kw == 0) { poll_first<15>(a3); poll<15, 6>(a3, want, 0, cnt); arrive<C_GO>(vl0, one); } else waitn<C_GO, 2, 0>(vl0);
      u32x4 e0 = ld<AUX_SC1>(rw, oo), e1 = ld<AUX_SC1>(rw, oo + 1024), e2 = ld<AUX_SC1>(rw, oo + 2048), e3 = ld<AUX_SC1>(rw, oo + 3072);
      if (EPE) __builtin_amdgcn_raw_buffer_store_b32(tag, rw, (cg == 0 && lane == 0 && cnt <= HINT_MAX) ? 0 : WS_END, 0, AUX_SC1);
      RETRY(BAD(unseal(e0, tb) | unseal(e1, tb) | unseal(e2, tb) | unseal(e3, tb)),
            (e0 = ld<AUX_SC1>(rw, oo), e1 = ld<AUX_SC1>(rw, oo + 1024), e2 = ld<AUX_SC1>(rw, oo + 2048), e3 = ld<AUX_SC1>(rw, oo + 3072)))
      const f32x4 e = (asf(e0) + asf(e1)) + (asf(e2) + asf(e3));
      auto s0 = __builtin_amdgcn_permlane32_swap(F2U(e[0]), F2U(e[2]), false, false), s1 = __builtin_amdgcn_permlane32_swap(F2U(e[1]), F2U(e[3]), false, false);
      if (WIDE) {
        float f0_ = U2F(s0[0]) + U2F(s0[1]), f1_ = U2F(s1[0]) + U2F(s1[1]);
        f0_ = x16_sum(f0_); f1_ = x16_sum(f1_);
        f0_ += dpp<ROR8>(f0_); f1_ += dpp<ROR8>(f1_); f0_ += dpp<ROR4>(f0_); f1_ += dpp<ROR4>(f1_);
        __builtin_amdgcn_raw_buffer_store_b32(pk(f0_, f1_), mk(out, 4096), oe, 0, OAUX);
      } else {
        auto s2 = __builtin_amdgcn_permlane16_swap(F2U(U2F(s0[0]) + U2F(s0[1])), F2U(U2F(s1[0]) + U2F(s1[1])), false, false);
        float f = U2F(s2[0]) + U2F(s2[1]);
        f += dpp<ROR8>(f); f += dpp<ROR4>(f);
        gate(t0 + 1600);
        __builtin_amdgcn_raw_buffer_store_b16((u16)pk(f, f), mk(out, 4096), oe, 0, OAUX);
      }
      if (cg == 0 && (!EPE || cnt > HINT_MAX)) __builtin_amdgcn_raw_buffer_store_b32(tag, rw, lane == 0 ? 0 : WS_END, 0, AUX_SC1);
    }
    return;
  }
  int mo = kvh * RG + WS_MB + ((p * 8 + q4) * 4 + (r >> 3)) * 16;
  if (wv < 4) {
    tag = get_tag(ws);
    const u32x4 tb = tag_bytes(tag);
    u32 wp = L0 + L_G1 + (8 * g + q4) * 128 + (((2 * wv + (p >> 1)) ^ ((((q4 >> 1) & 1) | ((g & 1) << 1)) << 1)) << 4) + 8 * (p & 1);
    u32 xp = L0 + L_X + p * XS + kq * 256 + g * 16;
    int dq = n >= 4 ? WS_END : kvh * RG + WS_QB + ((p * 4 + kq) * 128 + cq * 16 + 4 * wv + g) * 16;
    int df = n >= 4 ? WS_END : ((tau >> 3) & 7) * RG + WS_FB + ((((tau >> 6) * 8 + (tau & 7)) * 4 + p) * 16 + 4 * wv + g) * 16;
    PIN("+v"(wp), "+v"(xp), "+v"(dq), "+v"(df));
    if (has_f) asm volatile("s_waitcnt vmcnt(4)" ::: "memory"); else asm volatile("s_waitcnt vmcnt(0)" ::: "memory");
    arrive<C_G1>(vl0, one);
    waitn<C_G1, 4, 0>(vl0);
    u32x4 xf[4];
    {
      u32x4 wf[4];
#pragma unroll
      for (int ks = 0; ks < 4; ++ks) { wf[ks] = trfrag(wp + ks * 4096, 512); xf[ks] = lds<u32x4>(xp + ks * 64); }
      __builtin_amdgcn_sched_barrier(0);
      f32x4 acc = mfma(wf[0], xf[0], f32x4{0.f, 0.f, 0.f, 0.f}), acd = mfma(wf[1], xf[1], f32x4{0.f, 0.f, 0.f, 0.f});
      acc = mfma(wf[2], xf[2], acc); acd = mfma(wf[3], xf[3], acd);
      st16(rw, dq, seal_rn(acc + acd, tb));
    }
    if (has_f) {
      asm volatile("s_waitcnt vmcnt(1)" ::: "memory");
      arrive<C_F1>(vl0, one);
      waitn<C_F1, 4, 0>(vl0);
      u32x4 wf[4];
#pragma unroll
      for (int ks = 0; ks < 4; ++ks) wf[ks] = trfrag(wp + 16384 + ks * 4096, 512);
      __builtin_amdgcn_sched_barrier(0);
      f32x4 acc = mfma(wf[0], xf[0], f32x4{0.f, 0.f, 0.f, 0.f}), acd = mfma(wf[1], xf[1], f32x4{0.f, 0.f, 0.f, 0.f});
      acc = mfma(wf[2], xf[2], acc); acd = mfma(wf[3], xf[3], acd);
      st16(rw, df, seal_rn(acc + acd, tb));
    }
    if (wv == 0 && r < 8)
      __builtin_amdgcn_raw_buffer_store_b64(u32x2{0u, tag}, rw, lane == 0 ? r * RG + WS_C2 + kvh * 256 : lane == 1 ? c3_at(w) : WS_END, 0, AUX_SC1);
    {
      const int qo = kvh * RG + WS_QB + ((b * 4 + (lane >> 5)) * 128 + wv * 32 + (lane & 31)) * 16;
      gate((u32)__builtin_amdgcn_s_memrealtime() + DQ);
      gate(t0 + T1);
      u32x4 q0 = ld<AUX_SC1>(rw, qo), q1 = ld<AUX_SC1>(rw, qo + 4096);
      u32x4 gwv = ld<0>(mk(gw, 1024), lane * 16);
      RETRY(BAD(unseal(q0, tb) | unseal(q1, tb)), (q0 = ld<AUX_SC1>(rw, qo), q1 = ld<AUX_SC1>(rw, qo + 4096)))
      {
        const f32x4 qs = asf(q0) + asf(q1);
        auto s0 = __builtin_amdgcn_permlane32_swap(F2U(qs[0]), F2U(qs[2]), false, false), s1 = __builtin_amdgcn_permlane32_swap(F2U(qs[1]), F2U(qs[3]), false, false);
        sts<u32>(L0 + L_Q + wv * QS + (lane & 31) * 8 + (lane >> 5) * 4, pk(U2F(s0[0]) + U2F(s0[1]), U2F(s1[0]) + U2F(s1[1])));
      }
      arrive<C_Q>(vl0, one);
      PIN("+v"(gwv));
      const u32x4 xs = lds<u32x4>(L0 + L_X + wv * XS + lane * 16);
      float z = 0.f;
#pragma unroll
      for (int j = 0; j < 4; ++j) { const u32 t = pk(blo(xs[j]) * blo(gwv[j]), bhi(xs[j]) * bhi(gwv[j])); z += blo(t); z += bhi(t); }
      z += dpp<XOR1>(z); z += dpp<XOR2>(z); z += dpp<ROR4>(z); z += dpp<ROR8>(z); z = x16_sum(z); z = x32_sum(z);
      sts<float>(L0 + L_G + wv * 4, rbf(sigm(rbf(z))));
    }
    __builtin_amdgcn_s_setprio(0);
    {
      const u32 c1 = ((lane & 15) ^ (g << 1)) << 4;
      const u32 o0 = (kb * 64 + 16 * wv + g) * 1024 + nb * 256, o2 = (beta * 32 + 8 * wv + g) * 1024 + nb * 256 + (c1 ^ ((wv & 1) << 7));
      gate(t0 + G2T + wv * G2S);
      asm volatile("s_mov_b32 m0, %4\n\t" DMA4("s[80:81]")
                   :: "v"(o0 + c1), "v"(o0 + 4096 + c1), "v"(o0 + 8192 + (c1 ^ 128)), "v"(o0 + 12288 + (c1 ^ 128)), "s"(L0 + L_G2 + 4096 * wv) : "memory", "scc");
      if (has_w2)
        asm volatile("s_mov_b32 m0, %2\n\ts_nop 0\n\tglobal_load_lds_dwordx4 %0, s[82:83] nt\n\ts_add_u32 m0, m0, 0x400\n\ts_nop 0\n\tglobal_load_lds_dwordx4 %1, s[82:83] nt"
                     :: "v"(o2), "v"(o2 + 4096), "s"(L0 + L_G2 + 16384 + 2048 * wv) : "memory", "scc");
    }
    __builtin_amdgcn_s_setprio(2);
    arrive<C_V>(vl0, one);
    BAR();
    asm volatile("s_waitcnt vmcnt(0)" ::: "memory");
    arrive<C_G2>(vl0, one);
  } else {
    const int vw = wv - 8;
    __builtin_amdgcn_s_setprio(1);
    const rsrc_t rv = mk((const unsigned char*)vc + kvoff, 65536);
    BAR0();
    tag = get_tag(ws);
    u32x4 vf[4][4];
#pragma unroll
    for (int t = 0; t < 4; ++t) {
      const int tile = ((4 * t + wv - 4 + r) & 15) * 4096;
      gate(t0 + P0 + (4 + t) * PACE + vw * (PACE / 4));
#pragma unroll
      for (int i = 0; i < 4; ++i) vf[t][i] = ld<AUX_NT>(rv, tile + lane * 16 + i * 1024);
      __builtin_amdgcn_sched_barrier(0);
    }
    const int hn = (n + q4) & 3;
    u32 pra = L0 + L_P + vw * 512 + hn * 128 + g * 32, vlp = L0 + vw * 4;
    u32 pw = L0 + L_PO + vw * 2048 + g * 512 + n * 32;
    GAS u64* ap = (GAS u64*)(ws + (lane & 7) * RG + WS_C2 + kvh * 256);
    GAS u64* cp = (GAS u64*)(ws + (r & 7) * RG + WS_C2 + kvh * 256);
    u64 want = ((u64)tag << 32) | 32u, inc = 1;
    f32x4 acc[8];
    u32x4 va[8], vb[8];
#define PERM(t, i, c) __builtin_amdgcn_perm(vf[t][(i) + 1][(c) >> 1], vf[t][i][(c) >> 1], ((c) & 1) ? 0x07060302u : 0x05040100u)
#pragma unroll
    for (int c = 0; c < 8; ++c) va[c] = u32x4{PERM(0, 0, c), PERM(0, 2, c), PERM(1, 0, c), PERM(1, 2, c)};
    PIN("+v"(va[0]), "+v"(va[1]), "+v"(va[2]), "+v"(va[3]), "+v"(va[4]), "+v"(va[5]), "+v"(va[6]), "+v"(va[7]), "+v"(pra), "+v"(vlp), "+v"(pw));
    PIN("+v"(ap), "+v"(cp), "+v"(want), "+v"(inc), "+v"(mo));
    __builtin_amdgcn_sched_barrier(0);
#pragma unroll
    for (int c = 0; c < 8; ++c) { vb[c][0] = PERM(2, 0, c); vb[c][1] = PERM(2, 2, c); vb[c][2] = vb[c][0]; vb[c][3] = vb[c][0]; }
    PIN("+v"(vb[0]), "+v"(vb[1]), "+v"(vb[2]), "+v"(vb[3]), "+v"(vb[4]), "+v"(vb[5]), "+v"(vb[6]), "+v"(vb[7]));
    __builtin_amdgcn_s_setprio(3);
    waitn<C_P, 1, 1>(vlp);
    u32x4 pf0 = lds<u32x4>(pra), pf1 = lds<u32x4>(pra + 16);
#pragma unroll
    for (int c = 0; c < 8; ++c) acc[c] = mfma(pf0, va[c], f32x4{0.f, 0.f, 0.f, 0.f});
    __builtin_amdgcn_sched_barrier(0);
#pragma unroll
    for (int c = 0; c < 8; ++c) vb[c][2] = PERM(3, 0, c);
    PIN("+v"(vb[0]), "+v"(vb[1]), "+v"(vb[2]), "+v"(vb[3]), "+v"(vb[4]), "+v"(vb[5]), "+v"(vb[6]), "+v"(vb[7]), "+v"(pf1));
    __builtin_amdgcn_sched_barrier(0);
    PIN("+v"(vf[3][2]), "+v"(vf[3][3]));
    arrive<C_V>(vl0, one);
#pragma unroll
    for (int c = 0; c < 8; ++c) { vb[c][3] = PERM(3, 2, c); acc[c] = mfma(pf1, vb[c], acc[c]); }
    sts<f32x4>(pw, f32x4{acc[0][0], acc[1][0], acc[2][0], acc[3][0]});
    sts<f32x4>(pw + 16, f32x4{acc[4][0], acc[5][0], acc[6][0], acc[7][0]});
    if (vw == 0) {
      waitn<C_V, 12, 0>(vl0);
      count_up<255>(ap, inc);
      poll_first<1>(cp);
      BAR();
      u32 cnt = HINT_MAX;
      poll<1, 8>(cp, want, 20, cnt);
      arrive<C_GO>(vl0, one);
      asm volatile("global_load_dwordx4 v[152:155], %0, %1 sc1\n\tglobal_load_dwordx4 v[156:159], %0, %1 offset:256 sc1" :: "v"(mo), "s"(ws)
                   : "memory", "v152", "v153", "v154", "v155", "v156", "v157", "v158", "v159");
    } else {
      BAR();
    }
    __builtin_amdgcn_s_setprio(2);
  }
  const int ct = wv < 4 ? wv : wv - 4;
  u32 wa0 = L0 + L_G2 + (8 * g + q4) * 256 + (((2 * ct + (p >> 1)) ^ ((q4 | ((g & 1) << 2)) << 1)) << 4) + 8 * (p & 1);
  u32 orr = L0 + L_O + n * OS + g * 16, sa = L0 + L_S + n * 8;
  int oa = n >= 4 ? WS_END : ct * RG + WS_OB + (((4 * p + nb) * 64 + kb) * 4 + g) * 16;
  float gt = lds<float>(L0 + L_G + p * 4);
  const u32x4 hf = lds<u32x4>(L0 + L_H + p * HS + g * 16);
  const float og = n < 4 ? rbf(1.f - gt) : 0.f;
  u32x4 tb = tag_bytes(tag);
  waitn<C_G2, 4, 0>(vl0);
  f32x4 ab = {0.f, 0.f, 0.f, 0.f};
  if (has_w2) ab = mfma(trfrag(wa0 + 16384, 1024), hf, ab) * og;
  u32x4 wf0 = trfrag(wa0, 1024), wf1 = trfrag(wa0 + 8192, 1024);
  PIN("+v"(wf0), "+v"(wf1), "+v"(ab), "+v"(orr), "+v"(oa), "+v"(sa), "+v"(gt), "+v"(tb));
  if (wv == 8) {
    float ma, mb, la, lb; u32 bad;
    asm volatile("s_waitcnt vmcnt(0)\n\tv_xor_b32 %4, %5, v153\n\tv_mov_b32 %0, v152\n\tv_bitop3_b32 %4, %5, v157, %4 bitop3:0xbe\n\tv_mov_b32 %1, v156\n\t"
                 "v_bitop3_b32 %4, %6, v155, %4 bitop3:0xbe\n\tv_mov_b32 %2, v154\n\tv_bitop3_b32 %4, %6, v159, %4 bitop3:0xbe\n\tv_mov_b32 %3, v158"
                 : "=&v"(ma), "=&v"(mb), "=&v"(la), "=&v"(lb), "=&v"(bad) : "s"(tag), "s"(~tag) : "memory");
    if (__builtin_expect(__any(bad != 0), 0)) {
      _Pragma("nounroll") for (int sp = 0; sp < SPIN_MAX; ++sp) {
        const u32x4 m0 = ld<AUX_SC1>(rw, mo), m1 = ld<AUX_SC1>(rw, mo + 256);
        asm volatile("" ::: "memory");
        ma = U2F(m0[0]); mb = U2F(m1[0]); la = U2F(m0[2]); lb = U2F(m1[2]);
        if (!__any(((m0[1] ^ tag) | (m0[3] ^ ~tag) | (m1[1] ^ tag) | (m1[3] ^ ~tag)) != 0)) break;
      }
    }
    float M;
    asm volatile("v_max_f32 %0, %1, %2\n\ts_nop 1\n\tv_max_f32_dpp %0, %0, %0 row_ror:4 row_mask:0xf bank_mask:0xf\n\ts_nop 1\n\tv_max_f32_dpp %0, %0, %0 row_ror:8 row_mask:0xf bank_mask:0xf"
                 : "=&v"(M) : "v"(ma), "v"(mb));
    const float e0 = ex2(ma - M), e1 = ex2(mb - M);
    float Ls = e0 * la + e1 * lb; Ls += dpp<ROR4>(Ls); Ls += dpp<ROR8>(Ls);
    const float sc = gt * rcp(Ls);
    sts<f32x2>(sa, f32x2{e0 * sc, e1 * sc});
  }
  BAR();
  {
    const u32x4 a0 = lds<u32x4>(orr), a1 = lds<u32x4>(orr + 16 * OS), a2 = lds<u32x4>(orr + 64), a3 = lds<u32x4>(orr + 16 * OS + 64);
    const f32x2 sw = lds<f32x2>(sa);
    __builtin_amdgcn_sched_barrier(0);
    f32x4 d0 = mfma(wf0, a0, f32x4{0.f, 0.f, 0.f, 0.f}), d1 = mfma(wf0, a1, f32x4{0.f, 0.f, 0.f, 0.f});
    __builtin_amdgcn_sched_barrier(0);
    d0 = mfma(wf1, a2, d0); d1 = mfma(wf1, a3, d1);
    ab += sw[0] * d0; ab += sw[1] * d1;
  }
#pragma unroll
  for (int j = 0; j < 4; ++j) ab[j] += dpp<ROR4>(ab[j]);
#pragma unroll
  for (int j = 0; j < 4; ++j) ab[j] += dpp<ROR8>(ab[j]);
  st16(rw, oa, seal(ab, tb));
}

extern "C" void kernel_launch(void* const* d_in, const int* in_sizes, int n_in, void* d_out, int out_size, void* d_ws, size_t ws_size, hipStream_t stream) {
  fused_decode<<<256, 768, 0, stream>>>((const u16*)d_in[3], (const u16*)d_in[6], (const u16*)d_in[7], (const u16*)d_in[1], (const u16*)d_in[2], (const u16*)d_in[0],
      (unsigned char*)d_ws, (const u16*)d_in[9], (const u16*)d_in[4], (const u16*)d_in[8], (u16*)d_out);
}
```

```cpp
#include <hip/hip_runtime.h>
#include <stdint.h>

typedef unsigned short u16;
typedef unsigned u32;
typedef unsigned long long u64;
typedef u32 u32x4 __attribute__((ext_vector_type(4)));
typedef u32 u32x2 __attribute__((ext_vector_type(2)));
typedef float f32x4 __attribute__((ext_vector_type(4)));
typedef float f32x2 __attribute__((ext_vector_type(2)));
typedef short s16x4 __attribute__((ext_vector_type(4)));
typedef __bf16 bf16x8 __attribute__((ext_vector_type(8)));
typedef __bf16 bf16x2 __attribute__((ext_vector_type(2)));
#define LAS __attribute__((address_space(3)))
#define GAS __attribute__((address_space(1)))
typedef __amdgpu_buffer_rsrc_t rsrc_t;

constexpr int SK = 8192, WIN = 2048;
constexpr int PACE = 58;
constexpr int P0 = 80;
constexpr int G2T = P0 + 8 * PACE + 10, G2S = 3 * PACE / 8;
constexpr int T1 = 285, DQ = 85;
constexpr int RG = 266496;
constexpr int WS_QB = 4096, WS_FB = WS_QB + 32768, WS_MB = WS_FB + 32768, WS_C2 = WS_MB + 2048, WS_C3 = WS_C2 + 2048, WS_PB = WS_C3 + 2048,
              WS_OB = WS_PB + 65536, WS_END = 8 * RG;
static_assert(WS_OB + 65536 <= RG, "region map");
constexpr int L_PO = 0, L_P = 8192, L_X = L_P + 2048, XS = 1040, L_Q = L_X + 4 * XS, QS = 272, L_M = L_Q + 4 * QS, L_L = L_M + 64,
              L_O = L_L + 64, OS = 144, L_S = L_O + 32 * OS, L_H = L_S + 128, HS = 80, L_G = L_H + 4 * HS, L_CNT = L_G + 16,
              L_G2 = (L_CNT + 64 + 255) & ~255, L_G1 = L_G2 + 24576, L_K = L_G1 + 32768, L_END = L_K + 65536;
constexpr int C_G1 = L_CNT, C_Q = L_CNT + 4, C_M = L_CNT + 8, C_V = L_CNT + 12, C_G2 = L_CNT + 16, C_GO = L_CNT + 20, C_F1 = L_CNT + 24,
              C_T0 = L_CNT + 28, C_P = L_CNT + 32;
constexpr int AUX_NT = 2, AUX_SC1 = 16;
constexpr int SPIN_MAX = 1 << 20, HINT_MAX = 512;

#define PIN(...) asm volatile("" : __VA_ARGS__)

__device__ __forceinline__ int c3_at(int j) { return (j & 7) * RG + WS_C3 + (j >> 3) * 256; }
__device__ __forceinline__ void gate(u32 until) {
  asm volatile("1:\n\ts_memrealtime vcc\n\ts_waitcnt lgkmcnt(0)\n\ts_sub_i32 vcc_lo, vcc_lo, %0\n\ts_cmp_ge_i32 vcc_lo, 0\n\ts_cbranch_scc1 2f\n\ts_sleep 1\n\ts_branch 1b\n2:"
               :: "s"(until) : "memory", "vcc", "scc");
}
__device__ __forceinline__ rsrc_t mk(const void* p, u32 n) { return __builtin_amdgcn_make_buffer_rsrc((void*)p, 0, n, 0x00020000); }
template <int AUX> __device__ __forceinline__ u32x4 ld(rsrc_t r, int off) { return __builtin_amdgcn_raw_buffer_load_b128(r, off, 0, AUX); }
__device__ __forceinline__ void st16(rsrc_t r, int off, u32x4 v) { __builtin_amdgcn_raw_buffer_store_b128(v, r, off, 0, AUX_SC1); }
#define PISSUE "global_atomic_add_x2 v[160:161], %[p], %[z], off sc0 sc1\n\t"
#define PCHECK "s_sleep 1\n\tv_cmp_ne_u64 vcc, v[160:161], %[want]\n\ts_cbranch_vccz 2f\n\t"
template <int MASK> __device__ __forceinline__ void poll_first(GAS u64* p) {
  u64 sv; u64 zero = 0;
  asm volatile("s_mov_b64 %[sv], exec\n\ts_mov_b64 exec, %[mask]\n\tv_mov_b64 v[160:161], -1\n\t" PISSUE "s_mov_b64 exec, %[sv]"
               : [sv] "=&s"(sv) : [p] "v"(p), [z] "v"(zero), [mask] "i"(MASK) : "memory", "v160", "v161");
}
template <int MASK, int DEPTH> __device__ __forceinline__ void poll(GAS u64* p, u64 want, u32 thr, u32& cnt) {
  u64 sv; u64 zero = 0;
  asm volatile("s_mov_b64 %[sv], exec\n\ts_mov_b64 exec, %[mask]\n"
               "1:\n\t" PCHECK PCHECK PCHECK
               "v_cmp_gt_u32 vcc, %[thr], v160\n\ts_cbranch_vccz 3f\n\ts_sleep 6\n"
               "3:\n\ts_waitcnt vmcnt(%[depth])\n\tv_cmp_ne_u64 vcc, v[160:161], %[want]\n\ts_cbranch_vccz 2f\n\t" PISSUE "s_sub_u32 %[cnt], %[cnt], 1\n\ts_cbranch_scc0 1b\n"
               "2:\n\ts_mov_b64 exec, %[sv]"
               : [sv] "=&s"(sv), [cnt] "+s"(cnt) : [p] "v"(p), [z] "v"(zero), [want] "v"(want), [thr] "s"(thr), [mask] "i"(MASK), [depth] "i"(DEPTH - 1)
               : "memory", "vcc", "scc", "v160", "v161");
}
template <int MASK> __device__ __forceinline__ void count_up(GAS u64* p, u64 inc) {
  u64 sv;
  asm volatile("s_mov_b64 %0, exec\n\ts_mov_b64 exec, %3\n\tglobal_atomic_add_x2 %1, %2, off sc1\n\ts_mov_b64 exec, %0" : "=&s"(sv) : "v"(p), "v"(inc), "i"(MASK) : "memory");
}
__device__ __forceinline__ f32x4 mfma(u32x4 a, u32x4 b, f32x4 c) {
  return __builtin_amdgcn_mfma_f32_16x16x32_bf16(__builtin_bit_cast(bf16x8, a), __builtin_bit_cast(bf16x8, b), c, 0, 0, 0);
}
__device__ __forceinline__ u32x4 trfrag(u32 a, int rowbytes4) {
  s16x4 x = __builtin_amdgcn_ds_read_tr16_b64_v4i16((LAS s16x4*)(uintptr_t)a);
  s16x4 y = __builtin_amdgcn_ds_read_tr16_b64_v4i16((LAS s16x4*)(uintptr_t)(a + rowbytes4));
  u32x2 u = __builtin_bit_cast(u32x2, x), v = __builtin_bit_cast(u32x2, y);
  return u32x4{u[0], u[1], v[0], v[1]};
}
template <typename T> __device__ __forceinline__ T lds(u32 a) { return *(LAS T*)(uintptr_t)a; }
template <typename T> __device__ __forceinline__ void sts(u32 a, T v) { *(LAS T*)(uintptr_t)a = v; }
__device__ __forceinline__ float U2F(u32 x) { return __uint_as_float(x); }
__device__ __forceinline__ u32 F2U(float x) { return __float_as_uint(x); }
__device__ __forceinline__ float rbf(float f) { return (float)(__bf16)f; }
__device__ __forceinline__ u32 pk(float lo, float hi) { bf16x2 v = {(__bf16)lo, (__bf16)hi}; return __builtin_bit_cast(u32, v); }
__device__ __forceinline__ float blo(u32 w) { return U2F(w << 16); }
__device__ __forceinline__ float bhi(u32 w) { return U2F(w & 0xffff0000u); }
__device__ __forceinline__ float rcp(float x) { return __builtin_amdgcn_rcpf(x); }
__device__ __forceinline__ float ex2(float x) { return __builtin_amdgcn_exp2f(x); }
__device__ __forceinline__ float sigm(float z) { return rcp(1.f + ex2(-1.4426950408889634f * z)); }
template <int CTRL> __device__ __forceinline__ float dpp(float x) {
  return __builtin_bit_cast(float, __builtin_amdgcn_mov_dpp(__builtin_bit_cast(int, x), CTRL, 0xf, 0xf, true));
}
constexpr int XOR1 = 0xB1, XOR2 = 0x4E, ROR4 = 0x124, ROR8 = 0x128;
__device__ __forceinline__ float x16_sum(float x) { auto s = __builtin_amdgcn_permlane16_swap(F2U(x), F2U(x), false, false); return U2F(s[0]) + U2F(s[1]); }
__device__ __forceinline__ float x32_sum(float x) { auto s = __builtin_amdgcn_permlane32_swap(F2U(x), F2U(x), false, false); return U2F(s[0]) + U2F(s[1]); }
__device__ __forceinline__ float x16_max(float x) { auto s = __builtin_amdgcn_permlane16_swap(F2U(x), F2U(x), false, false); return fmaxf(U2F(s[0]), U2F(s[1])); }
__device__ __forceinline__ float x32_max(float x) { auto s = __builtin_amdgcn_permlane32_swap(F2U(x), F2U(x), false, false); return fmaxf(U2F(s[0]), U2F(s[1])); }
__device__ __forceinline__ u32x4 tag_bytes(u32 tag) { return u32x4{tag & 0xff, (tag >> 8) & 0xff, (tag >> 16) & 0xff, tag >> 24}; }
__device__ __forceinline__ u32x4 seal(f32x4 v, u32x4 tb) {
  return u32x4{(F2U(v[0]) & 0xffffff00u) | tb[0], (F2U(v[1]) & 0xffffff00u) | tb[1], (F2U(v[2]) & 0xffffff00u) | tb[2], (F2U(v[3]) & 0xffffff00u) | tb[3]};
}
__device__ __forceinline__ u32x4 seal_rn(f32x4 v, u32x4 tb) {
  return seal(__builtin_bit_cast(f32x4, __builtin_bit_cast(u32x4, v) + 0x80u), tb);
}
__device__ __forceinline__ u32 unseal(u32x4& v, u32x4 tb) { v ^= tb; return v[0] | v[1] | v[2] | v[3]; }
#define BAD(x) __any(((x) & 0xff) != 0)
__device__ __forceinline__ f32x4 asf(u32x4 v) { return __builtin_bit_cast(f32x4, v); }
__device__ __forceinline__ u32 get_tag(const unsigned char* ws) {
  u32 ep;
  asm volatile("s_load_dword %0, %1, 0x0\n\ts_waitcnt lgkmcnt(0)" : "=s"(ep) : "s"(ws) : "memory");
  return ep + 0x3b5d2f47u;
}
#define BAR() asm volatile("s_waitcnt lgkmcnt(0)\n\ts_barrier" ::: "memory")
#define BAR0() do { BAR(); t0 = __builtin_amdgcn_readfirstlane(lds<u32>(L0 + C_T0)); } while (0)
template <int OFF> __device__ __forceinline__ void arrive(u32 l0, u32 one) {
  u64 sv;
  asm volatile("s_mov_b64 %0, exec\n\ts_mov_b64 exec, 1\n\tds_add_u32 %1, %2 offset:%3\n\ts_mov_b64 exec, %0" : "=&s"(sv) : "v"(l0), "v"(one), "i"(OFF) : "memory");
}
template <int OFF, int N, int SLEEP> __device__ __forceinline__ void waitn(u32 l0) {
  u32 t;
  asm volatile("s_branch 1f\n0:\n\ts_sleep %4\n1:\n\tds_read_b32 %0, %1 offset:%2\n\ts_waitcnt lgkmcnt(0)\n\tv_cmp_ne_u32 vcc, %3, %0\n\ts_cbranch_vccnz 0b"
               : "=&v"(t) : "v"(l0), "i"(OFF), "i"(N), "i"(SLEEP) : "memory", "vcc");
}
#define RETRY(cond, reload) \
  if (__builtin_expect(cond, 0)) { _Pragma("nounroll") for (int sp = 0; sp < SPIN_MAX; ++sp) { reload; asm volatile("" ::: "memory"); if (!(cond)) break; } }

__global__ void __launch_bounds__(768, 3) fused_decode(const u16*, const u16*, const u16*, const u16*, const u16*, const u16*, unsigned char*,
                                                       const u16*, const u16*, const u16*, u16*) {
  asm volatile("s_load_dwordx16 s[64:79], %0, 0x0\n\ts_load_dwordx4 s[80:83], %0, 0x40\n\ts_load_dwordx2 s[84:85], %0, 0x50"
               :: "s"(__builtin_amdgcn_kernarg_segment_ptr())
               : "memory", "s64", "s65", "s66", "s67", "s68", "s69", "s70", "s71", "s72", "s73", "s74", "s75", "s76", "s77", "s78", "s79", "s80", "s81",
                 "s82", "s83", "s84", "s85");
  __shared__ __attribute__((aligned(256))) unsigned char smem[L_END];
  const u32 L0 = (u32)(uintptr_t)(LAS unsigned char*)smem;
  const int w = blockIdx.x, tid = threadIdx.x, lane = tid & 63;
  int wv = __builtin_amdgcn_readfirstlane(tid >> 6);
  const int kvh = w & 7, r = w >> 3, b = r >> 3, s = r & 7;
  const int n = lane & 15, g = lane >> 4, q4 = n >> 2, p = n & 3;
  const int kq = r & 3, tau = kvh * 28 + r, cq = ((r >> 2) + 4) & 7;
  const bool has_f = r < 28;
  const u32 kvoff = (u32)(((b * 8 + kvh) * SK + (SK - WIN) + s * 256) * 256);
  u32 t0 = 0;
#define DMA4(base) "s_nop 0\n\tglobal_load_lds_dwordx4 %0, " base " nt\n\ts_add_u32 m0, m0, 0x400\n\ts_nop 0\n\tglobal_load_lds_dwordx4 %1, " base " nt\n\t" \
                   "s_add_u32 m0, m0, 0x400\n\ts_nop 0\n\tglobal_load_lds_dwordx4 %2, " base " nt\n\ts_add_u32 m0, m0, 0x400\n\ts_nop 0\n\tglobal_load_lds_dwordx4 %3, " base " nt"
  if (wv >= 8) {
  } else if (wv >= 4) {
    __builtin_amdgcn_s_setprio(2);
    const u32 k0 = kvoff + g * 256 + ((n ^ (4 * g)) << 4);
    BAR0();
#pragma unroll
    for (int t = 0; t < 4; ++t) {
      const u32 o = k0 + ((4 * t + wv + r) & 15) * 4096;
      gate(t0 + P0 + t * PACE + (wv - 4) * (PACE / 4));
      asm volatile("s_mov_b32 m0, %4\n\ts_nop 0\n\tglobal_load_lds_dwordx4 %0, s[70:71] nt\n\tglobal_load_lds_dwordx4 %1, s[70:71] offset:1024 nt\n\t"
                   "global_load_lds_dwordx4 %2, s[70:71] offset:2048 nt\n\tglobal_load_lds_dwordx4 %3, s[70:71] offset:3072 nt"
                   :: "v"(o), "v"(o ^ 16), "v"(o ^ 32), "v"(o ^ 48), "s"(L0 + L_K + (wv - 4) * 16384 + t * 4096) : "memory");
    }
  } else {
    const u32 row = 128 * kq + 32 * wv + (lane >> 3), c0 = ((lane & 7) ^ (((lane >> 4) & 1) << 1)) << 4;
    const u32 oq0 = row * 8192 + kvh * 1024 + cq * 128 + c0, oq1 = (oq0 ^ 64) + 8 * 8192;
    u32 of0 = row * 3584 + (tau >> 3) * 128 + c0, of1 = (of0 ^ 64) + 8 * 3584, of2 = of0 + 16 * 3584, of3 = of1 + 16 * 3584;
    u32 mf = L0 + L_G1 + 16384 + 4096 * wv;
    PIN("+v"(of0), "+v"(of1), "+v"(of2), "+v"(of3), "+s"(mf));
    asm volatile("s_mov_b32 m0, %4\n\ts_waitcnt lgkmcnt(0)\n\t" DMA4("s[64:65]") "\n\ts_mov_b32 m0, %6\n\ts_nop 0\n\tglobal_load_lds_dwordx4 %5, s[74:75]"
                 :: "v"(oq0), "v"(oq1), "v"(oq0 + 16 * 8192), "v"(oq1 + 16 * 8192), "s"(L0 + L_G1 + 4096 * wv), "v"(tid * 16), "s"(L0 + L_X + wv * XS) : "memory", "scc");
    if (has_f)
      asm volatile("s_bitcmp1_b32 %5, 2\n\ts_cselect_b64 s[66:67], s[68:69], s[66:67]\n\ts_mov_b32 m0, %4\n\t" DMA4("s[66:67]")
                   :: "v"(of0), "v"(of1), "v"(of2), "v"(of3), "s"(mf), "s"(tau) : "memory", "scc");
    __builtin_amdgcn_s_setprio(3);
    if (tid < 16) sts<u32>(L0 + L_CNT + tid * 4, tid == 7 ? (u32)__builtin_amdgcn_s_memrealtime() : 0u);
    BAR0();
  }
  const u16 *vc, *gw; unsigned char* ws; u16* out;
  asm volatile("s_waitcnt lgkmcnt(0)\n\ts_mov_b64 %0, s[72:73]\n\ts_mov_b64 %1, s[76:77]\n\ts_mov_b64 %2, s[78:79]\n\ts_mov_b64 %3, s[84:85]"
               : "=s"(vc), "=s"(ws), "=s"(gw), "=s"(out) :: "memory");
  asm volatile("" : "+s"(wv));
  u32 tag;
  const rsrc_t rw = mk(ws, WS_END);
  const int nb = r & 3, kb = 8 * kvh + (r >> 2), beta = 7 * kvh + (r >> 2);
  const bool has_w2 = (r >> 2) < 7;
  u32 vl0 = L0, one = 1; PIN("+v"(vl0), "+v"(one));

  if (wv >= 4 && wv < 8) {
    const int kw = wv - 4;
    tag = get_tag(ws);
    u32x4 tb = tag_bytes(tag);
    asm volatile("s_waitcnt vmcnt(0)" ::: "memory");
    const int bt = has_w2 ? beta : 0;
    const int fo = ((bt >> 1) & 7) * RG + WS_FB + ((((bt >> 4) * 8 + (lane >> 3)) * 4 + kw) * 16 + (bt & 1) * 8 + (lane & 7)) * 16;
    u32x4 f0 = ld<AUX_SC1>(rw, fo);
    u32x4 kf[4][4];
    {
      const u32 ka = L0 + L_K + kw * 16384 + (4 * p + q4) * 256 + ((g ^ n) << 4);
#pragma unroll
      for (int i = 0; i < 4; ++i)
#pragma unroll
        for (int t = 0; t < 4; ++t) kf[t][i] = lds<u32x4>((ka ^ (i << 6)) + t * 4096);
    }
    u32 qa = L0 + L_Q + p * QS + g * 16, pwa = L0 + L_P + kw * 512 + p * 128 + g * 32 + q4 * 8, vlp = L0 + kw * 4;
#pragma unroll
    for (int t = 0; t < 4; ++t) PIN("+v"(kf[t][0]), "+v"(kf[t][1]), "+v"(kf[t][2]), "+v"(kf[t][3]));
    PIN("+v"(qa), "+v"(pwa), "+v"(vlp));
    waitn<C_Q, 4, 1>(vl0);
    u32x4 qf[4];
#pragma unroll
    for (int i = 0; i < 4; ++i) qf[i] = lds<u32x4>(qa + i * 64);
    f32x4 sc[4];
#pragma unroll
    for (int t = 0; t < 4; ++t) sc[t] = mfma(kf[t][0], qf[0], f32x4{0.f, 0.f, 0.f, 0.f});
#pragma unroll
    for (int i = 1; i < 4; ++i)
#pragma unroll
      for (int t = 0; t < 4; ++t) sc[t] = mfma(kf[t][i], qf[i], sc[t]);
    float v[4];
#pragma unroll
    for (int j = 0; j < 4; ++j) { const float lo = (n & 4) ? sc[1][j] : sc[0][j], hi = (n & 4) ? sc[3][j] : sc[2][j]; v[j] = (n & 8) ? hi : lo; }
#pragma unroll
    for (int j = 0; j < 4; j += 2) {
      u32 t = pk(v[j], v[j + 1]);
      t = pk(blo(t) * 0.08838834764831845f, bhi(t) * 0.08838834764831845f);
      v[j] = blo(t) * 1.4426950408889634f; v[j + 1] = bhi(t) * 1.4426950408889634f;
    }
    float m = fmaxf(fmaxf(v[0], v[1]), fmaxf(v[2], v[3]));
    m = fmaxf(m, dpp<ROR4>(m)); m = fmaxf(m, dpp<ROR8>(m)); m = x16_max(m); m = x32_max(m);
#pragma unroll
    for (int j = 0; j < 4; ++j) v[j] = ex2(v[j] - m);
    sts<u32x2>(pwa, u32x2{pk(v[0], v[1]), pk(v[2], v[3])});
    arrive<C_P>(vlp, one);
    float l = (v[0] + v[1]) + (v[2] + v[3]);
    l += dpp<ROR4>(l); l += dpp<ROR8>(l); l = x16_sum(l); l = x32_sum(l);
    sts<float>(L0 + L_M + p * 16 + kw * 4, m);
    sts<float>(L0 + L_L + p * 16 + kw * 4, l);
    arrive<C_M>(vl0, one);
    asm volatile("" : "+v"(f0) :: "memory");
    RETRY(BAD(unseal(f0, tb)), f0 = ld<AUX_SC1>(rw, fo))
    {
      f32x4 a = asf(f0);
#pragma unroll
      for (int j = 0; j < 4; ++j) { a[j] += dpp<ROR8>(a[j]); a[j] = x16_sum(a[j]); }
      const u32 u0 = pk(a[0], a[1]), u1 = pk(a[2], a[3]);
      auto s0 = __builtin_amdgcn_permlane32_swap(u0, u0, false, false), s1 = __builtin_amdgcn_permlane32_swap(u1, u1, false, false);
      const u32 g0 = pk(blo(s0[0]) * sigm(blo(s0[0])), bhi(s0[0]) * sigm(bhi(s0[0]))), g1 = pk(blo(s1[0]) * sigm(blo(s1[0])), bhi(s1[0]) * sigm(bhi(s1[0])));
      sts<u32x2>(L0 + L_H + kw * HS + (lane & 7) * 8, u32x2{pk(blo(g0) * blo(s0[1]), bhi(g0) * bhi(s0[1])), pk(blo(g1) * blo(s1[1]), bhi(g1) * bhi(s1[1]))});
    }
    waitn<C_M, 4, 1>(vl0);
    f32x4 al; u32x4 mu;
    {
      const int hs = lane < 32 ? kw : p;
      const f32x4 mm = lds<f32x4>(L0 + L_M + hs * 16), ll = lds<f32x4>(L0 + L_L + hs * 16);
      const float M = fmaxf(fmaxf(mm[0], mm[1]), fmaxf(mm[2], mm[3]));
#pragma unroll
      for (int j = 0; j < 4; ++j) al[j] = ex2(mm[j] - M);
      mu = u32x4{F2U(M), tag, F2U((al[0] * ll[0] + al[1] * ll[1]) + (al[2] * ll[2] + al[3] * ll[3])), ~tag};
    }
    u32 keep = lane < 32 ? 0xffffff00u : 0u;
    if (lane < 32) mu = tb;
    int pa = lane < 32 ? kvh * RG + WS_PB + ((((kw * 2 + (lane >> 4)) * 4 + b) * 8 + s) * 16 + n) * 16 : (kw == 1 && lane < 36) ? kvh * RG + WS_MB + (r * 4 + p) * 16 : WS_END;
    u32 pr = L0 + L_PO + kw * 512 + (lane & 31) * 16;
    int po = kvh * RG + WS_PB + (((r >> 2) * 4 + kw) * 128 + lane) * 16;
    u32 ow = L0 + L_O + (g * 4 + kw) * OS + n * 8;
    u64 inc = 1;
    GAS u64* a3 = (GAS u64*)(ws + c3_at(kw == 0 ? ((kvh >> 1) * 4 + (lane & 3)) * 4 + (r & 3) : (nb * 4 + (kvh >> 1)) * 4 + (lane & 3)));
    const int cg = 4 * w + kw;
    int oo = (cg & 7) * RG + WS_OB + ((cg >> 3) * 256 + lane) * 16;
    int oe = n < 4 ? (cg * 16 + 4 * p + g) * 2 : 4096;
    u64 want = ((u64)tag << 32) | 16u;
    PIN("+v"(pa), "+v"(pr), "+v"(po), "+v"(mu), "+v"(keep), "+v"(ow), "+v"(inc), "+v"(al), "+v"(a3), "+v"(oo), "+v"(oe), "+v"(want), "+v"(tb));
    __builtin_amdgcn_s_setprio(3);
    arrive<C_V>(vl0, one);
    BAR();
    {
      const f32x4 o = (al[0] * lds<f32x4>(pr) + al[1] * lds<f32x4>(pr + 2048)) + (al[2] * lds<f32x4>(pr + 4096) + al[3] * lds<f32x4>(pr + 6144));
      st16(rw, pa, u32x4{(F2U(o[0]) & keep) | mu[0], (F2U(o[1]) & keep) | mu[1], (F2U(o[2]) & keep) | mu[2], (F2U(o[3]) & keep) | mu[3]});
    }
    waitn<C_GO, 1, 0>(vl0);
    u32x4 p0 = ld<AUX_SC1>(rw, po), p1 = ld<AUX_SC1>(rw, po + 1024);
    RETRY(BAD(unseal(p0, tb) | unseal(p1, tb)), (p0 = ld<AUX_SC1>(rw, po), p1 = ld<AUX_SC1>(rw, po + 1024)))
    sts<u32x2>(ow, u32x2{pk(U2F(p0[0]), U2F(p0[1])), pk(U2F(p0[2]), U2F(p0[3]))});
    sts<u32x2>(ow + 16 * OS, u32x2{pk(U2F(p1[0]), U2F(p1[1])), pk(U2F(p1[2]), U2F(p1[3]))});
    BAR();
    if (kw == 1) count_up<15>(a3, inc);
    if (w < 32) {
      u32 cnt = HINT_MAX;
      if (kw == 0) { poll_first<15>(a3); poll<15, 6>(a3, want, 0, cnt); arrive<C_GO>(vl0, one); } else waitn<C_GO, 2, 0>(vl0);
      u32x4 e0 = ld<AUX_SC1>(rw, oo), e1 = ld<AUX_SC1>(rw, oo + 1024), e2 = ld<AUX_SC1>(rw, oo + 2048), e3 = ld<AUX_SC1>(rw, oo + 3072);
      u32 open;
      asm volatile("v_cmp_ne_u64 vcc, v[160:161], %1\n\ts_and_b32 %0, vcc_lo, 15" : "=s"(open) : "v"(want) : "vcc", "scc");
      const bool full = cnt <= HINT_MAX && open == 0;
      __builtin_amdgcn_raw_buffer_store_b32(tag, rw, (cg == 0 && lane == 0 && full) ? 0 : WS_END, 0, AUX_SC1);
      RETRY(BAD(unseal(e0, tb) | unseal(e1, tb) | unseal(e2, tb) | unseal(e3, tb)),
            (e0 = ld<AUX_SC1>(rw, oo), e1 = ld<AUX_SC1>(rw, oo + 1024), e2 = ld<AUX_SC1>(rw, oo + 2048), e3 = ld<AUX_SC1>(rw, oo + 3072)))
      const f32x4 e = (asf(e0) + asf(e1)) + (asf(e2) + asf(e3));
      auto s0 = __builtin_amdgcn_permlane32_swap(F2U(e[0]), F2U(e[2]), false, false), s1 = __builtin_amdgcn_permlane32_swap(F2U(e[1]), F2U(e[3]), false, false);
      auto s2 = __builtin_amdgcn_permlane16_swap(F2U(U2F(s0[0]) + U2F(s0[1])), F2U(U2F(s1[0]) + U2F(s1[1])), false, false);
      float f = U2F(s2[0]) + U2F(s2[1]);
      f += dpp<ROR8>(f); f += dpp<ROR4>(f);
      __builtin_amdgcn_raw_buffer_store_b16((u16)pk(f, f), mk(out, 4096), oe, 0, 0);
      if (cg == 0 && !full) __builtin_amdgcn_raw_buffer_store_b32(tag, rw, lane == 0 ? 0 : WS_END, 0, AUX_SC1);
    }
    return;
  }
  int mo = kvh * RG + WS_MB + ((p * 8 + q4) * 4 + (r >> 3)) * 16;
  if (wv < 4) {
    tag = get_tag(ws);
    const u32x4 tb = tag_bytes(tag);
    u32 wp = L0 + L_G1 + (8 * g + q4) * 128 + (((2 * wv + (p >> 1)) ^ ((((q4 >> 1) & 1) | ((g & 1) << 1)) << 1)) << 4) + 8 * (p & 1);
    u32 xp = L0 + L_X + p * XS + kq * 256 + g * 16;
    int dq = n >= 4 ? WS_END : kvh * RG + WS_QB + ((p * 4 + kq) * 128 + cq * 16 + 4 * wv + g) * 16;
    int df = n >= 4 ? WS_END : ((tau >> 3) & 7) * RG + WS_FB + ((((tau >> 6) * 8 + (tau & 7)) * 4 + p) * 16 + 4 * wv + g) * 16;
    PIN("+v"(wp), "+v"(xp), "+v"(dq), "+v"(df));
    if (has_f) asm volatile("s_waitcnt vmcnt(4)" ::: "memory"); else asm volatile("s_waitcnt vmcnt(0)" ::: "memory");
    arrive<C_G1>(vl0, one);
    waitn<C_G1, 4, 0>(vl0);
    u32x4 xf[4];
    {
      u32x4 wf[4];
#pragma unroll
      for (int ks = 0; ks < 4; ++ks) { wf[ks] = trfrag(wp + ks * 4096, 512); xf[ks] = lds<u32x4>(xp + ks * 64); }
      __builtin_amdgcn_sched_barrier(0);
      f32x4 acc = mfma(wf[0], xf[0], f32x4{0.f, 0.f, 0.f, 0.f}), acd = mfma(wf[1], xf[1], f32x4{0.f, 0.f, 0.f, 0.f});
      acc = mfma(wf[2], xf[2], acc); acd = mfma(wf[3], xf[3], acd);
      st16(rw, dq, seal_rn(acc + acd, tb));
    }
    if (has_f) {
      asm volatile("s_waitcnt vmcnt(1)" ::: "memory");
      arrive<C_F1>(vl0, one);
      waitn<C_F1, 4, 0>(vl0);
      u32x4 wf[4];
#pragma unroll
      for (int ks = 0; ks < 4; ++ks) wf[ks] = trfrag(wp + 16384 + ks * 4096, 512);
      __builtin_amdgcn_sched_barrier(0);
      f32x4 acc = mfma(wf[0], xf[0], f32x4{0.f, 0.f, 0.f, 0.f}), acd = mfma(wf[1], xf[1], f32x4{0.f, 0.f, 0.f, 0.f});
      acc = mfma(wf[2], xf[2], acc); acd = mfma(wf[3], xf[3], acd);
      st16(rw, df, seal_rn(acc + acd, tb));
    }
    if (wv == 0 && r < 8)
      __builtin_amdgcn_raw_buffer_store_b64(u32x2{0u, tag}, rw, lane == 0 ? r * RG + WS_C2 + kvh * 256 : lane == 1 ? c3_at(w) : WS_END, 0, AUX_SC1);
    {
      const int qo = kvh * RG + WS_QB + ((b * 4 + (lane >> 5)) * 128 + wv * 32 + (lane & 31)) * 16;
      gate((u32)__builtin_amdgcn_s_memrealtime() + DQ);
      gate(t0 + T1);
      u32x4 q0 = ld<AUX_SC1>(rw, qo), q1 = ld<AUX_SC1>(rw, qo + 4096);
      u32x4 gwv = ld<0>(mk(gw, 1024), lane * 16);
      RETRY(BAD(unseal(q0, tb) | unseal(q1, tb)), (q0 = ld<AUX_SC1>(rw, qo), q1 = ld<AUX_SC1>(rw, qo + 4096)))
      {
        const f32x4 qs = asf(q0) + asf(q1);
        auto s0 = __builtin_amdgcn_permlane32_swap(F2U(qs[0]), F2U(qs[2]), false, false), s1 = __builtin_amdgcn_permlane32_swap(F2U(qs[1]), F2U(qs[3]), false, false);
        sts<u32>(L0 + L_Q + wv * QS + (lane & 31) * 8 + (lane >> 5) * 4, pk(U2F(s0[0]) + U2F(s0[1]), U2F(s1[0]) + U2F(s1[1])));
      }
      arrive<C_Q>(vl0, one);
      PIN("+v"(gwv));
      const u32x4 xs = lds<u32x4>(L0 + L_X + wv * XS + lane * 16);
      float z = 0.f;
#pragma unroll
      for (int j = 0; j < 4; ++j) { const u32 t = pk(blo(xs[j]) * blo(gwv[j]), bhi(xs[j]) * bhi(gwv[j])); z += blo(t); z += bhi(t); }
      z += dpp<XOR1>(z); z += dpp<XOR2>(z); z += dpp<ROR4>(z); z += dpp<ROR8>(z); z = x16_sum(z); z = x32_sum(z);
      sts<float>(L0 + L_G + wv * 4, rbf(sigm(rbf(z))));
    }
    __builtin_amdgcn_s_setprio(0);
    {
      const u32 c1 = ((lane & 15) ^ (g << 1)) << 4;
      const u32 o0 = (kb * 64 + 16 * wv + g) * 1024 + nb * 256, o2 = (beta * 32 + 8 * wv + g) * 1024 + nb * 256 + (c1 ^ ((wv & 1) << 7));
      gate(t0 + G2T + wv * G2S);
      asm volatile("s_mov_b32 m0, %4\n\t" DMA4("s[80:81]")
                   :: "v"(o0 + c1), "v"(o0 + 4096 + c1), "v"(o0 + 8192 + (c1 ^ 128)), "v"(o0 + 12288 + (c1 ^ 128)), "s"(L0 + L_G2 + 4096 * wv) : "memory", "scc");
      if (has_w2)
        asm volatile("s_mov_b32 m0, %2\n\ts_nop 0\n\tglobal_load_lds_dwordx4 %0, s[82:83] nt\n\ts_add_u32 m0, m0, 0x400\n\ts_nop 0\n\tglobal_load_lds_dwordx4 %1, s[82:83] nt"
                     :: "v"(o2), "v"(o2 + 4096), "s"(L0 + L_G2 + 16384 + 2048 * wv) : "memory", "scc");
    }
    __builtin_amdgcn_s_setprio(2);
    arrive<C_V>(vl0, one);
    BAR();
    asm volatile("s_waitcnt vmcnt(0)" ::: "memory");
    arrive<C_G2>(vl0, one);
  } else {
    const int vw = wv - 8;
    __builtin_amdgcn_s_setprio(1);
    const rsrc_t rv = mk((const unsigned char*)vc + kvoff, 65536);
    BAR0();
    tag = get_tag(ws);
    u32x4 vf[4][4];
#pragma unroll
    for (int t = 0; t < 4; ++t) {
      const int tile = ((4 * t + wv - 4 + r) & 15) * 4096;
      gate(t0 + P0 + (4 + t) * PACE + vw * (PACE / 4));
#pragma unroll
      for (int i = 0; i < 4; ++i) vf[t][i] = ld<AUX_NT>(rv, tile + lane * 16 + i * 1024);
      __builtin_amdgcn_sched_barrier(0);
    }
    const int hn = (n + q4) & 3;
    u32 pra = L0 + L_P + vw * 512 + hn * 128 + g * 32, vlp = L0 + vw * 4;
    u32 pw = L0 + L_PO + vw * 2048 + g * 512 + n * 32;
    GAS u64* ap = (GAS u64*)(ws + (lane & 7) * RG + WS_C2 + kvh * 256);
    GAS u64* cp = (GAS u64*)(ws + (r & 7) * RG + WS_C2 + kvh * 256);
    u64 want = ((u64)tag << 32) | 32u, inc = 1;
    f32x4 acc[8];
    u32x4 va[8], vb[8];
#define PERM(t, i, c) __builtin_amdgcn_perm(vf[t][(i) + 1][(c) >> 1], vf[t][i][(c) >> 1], ((c) & 1) ? 0x07060302u : 0x05040100u)
#pragma unroll
    for (int c = 0; c < 8; ++c) va[c] = u32x4{PERM(0, 0, c), PERM(0, 2, c), PERM(1, 0, c), PERM(1, 2, c)};
    PIN("+v"(va[0]), "+v"(va[1]), "+v"(va[2]), "+v"(va[3]), "+v"(va[4]), "+v"(va[5]), "+v"(va[6]), "+v"(va[7]), "+v"(pra), "+v"(vlp), "+v"(pw));
    PIN("+v"(ap), "+v"(cp), "+v"(want), "+v"(inc), "+v"(mo));
    __builtin_amdgcn_sched_barrier(0);
#pragma unroll
    for (int c = 0; c < 8; ++c) { vb[c][0] = PERM(2, 0, c); vb[c][1] = PERM(2, 2, c); vb[c][2] = vb[c][0]; vb[c][3] = vb[c][0]; }
    PIN("+v"(vb[0]), "+v"(vb[1]), "+v"(vb[2]), "+v"(vb[3]), "+v"(vb[4]), "+v"(vb[5]), "+v"(vb[6]), "+v"(vb[7]));
    __builtin_amdgcn_s_setprio(3);
    waitn<C_P, 1, 1>(vlp);
    u32x4 pf0 = lds<u32x4>(pra), pf1 = lds<u32x4>(pra + 16);
#pragma unroll
    for (int c = 0; c < 8; ++c) acc[c] = mfma(pf0, va[c], f32x4{0.f, 0.f, 0.f, 0.f});
    __builtin_amdgcn_sched_barrier(0);
#pragma unroll
    for (int c = 0; c < 8; ++c) vb[c][2] = PERM(3, 0, c);
    PIN("+v"(vb[0]), "+v"(vb[1]), "+v"(vb[2]), "+v"(vb[3]), "+v"(vb[4]), "+v"(vb[5]), "+v"(vb[6]), "+v"(vb[7]), "+v"(pf1));
    __builtin_amdgcn_sched_barrier(0);
    PIN("+v"(vf[3][2]), "+v"(vf[3][3]));
    arrive<C_V>(vl0, one);
#pragma unroll
    for (int c = 0; c < 8; ++c) { vb[c][3] = PERM(3, 2, c); acc[c] = mfma(pf1, vb[c], acc[c]); }
    sts<f32x4>(pw, f32x4{acc[0][0], acc[1][0], acc[2][0], acc[3][0]});
    sts<f32x4>(pw + 16, f32x4{acc[4][0], acc[5][0], acc[6][0], acc[7][0]});
    if (vw == 0) {
      waitn<C_V, 12, 0>(vl0);
      count_up<255>(ap, inc);
      poll_first<1>(cp);
      BAR();
      u32 cnt = HINT_MAX;
      poll<1, 8>(cp, want, 20, cnt);
      arrive<C_GO>(vl0, one);
      asm volatile("global_load_dwordx4 v[152:155], %0, %1 sc1\n\tglobal_load_dwordx4 v[156:159], %0, %1 offset:256 sc1" :: "v"(mo), "s"(ws)
                   : "memory", "v152", "v153", "v154", "v155", "v156", "v157", "v158", "v159");
    } else {
      BAR();
    }
    __builtin_amdgcn_s_setprio(2);
  }
  const int ct = wv < 4 ? wv : wv - 4;
  u32 wa0 = L0 + L_G2 + (8 * g + q4) * 256 + (((2 * ct + (p >> 1)) ^ ((q4 | ((g & 1) << 2)) << 1)) << 4) + 8 * (p & 1);
  u32 orr = L0 + L_O + n * OS + g * 16, sa = L0 + L_S + n * 8;
  int oa = n >= 4 ? WS_END : ct * RG + WS_OB + (((4 * p + nb) * 64 + kb) * 4 + g) * 16;
  float gt = lds<float>(L0 + L_G + p * 4);
  const u32x4 hf = lds<u32x4>(L0 + L_H + p * HS + g * 16);
  const float og = n < 4 ? rbf(1.f - gt) : 0.f;
  u32x4 tb = tag_bytes(tag);
  waitn<C_G2, 4, 0>(vl0);
  f32x4 ab = {0.f, 0.f, 0.f, 0.f};
  if (has_w2) ab = mfma(trfrag(wa0 + 16384, 1024), hf, ab) * og;
  u32x4 wf0 = trfrag(wa0, 1024), wf1 = trfrag(wa0 + 8192, 1024);
  PIN("+v"(wf0), "+v"(wf1), "+v"(ab), "+v"(orr), "+v"(oa), "+v"(sa), "+v"(gt), "+v"(tb));
  if (wv == 8) {
    float ma, mb, la, lb; u32 bad;
    asm volatile("s_waitcnt vmcnt(0)\n\tv_xor_b32 %4, %5, v153\n\tv_mov_b32 %0, v152\n\tv_bitop3_b32 %4, %5, v157, %4 bitop3:0xbe\n\tv_mov_b32 %1, v156\n\t"
                 "v_bitop3_b32 %4, %6, v155, %4 bitop3:0xbe\n\tv_mov_b32 %2, v154\n\tv_bitop3_b32 %4, %6, v159, %4 bitop3:0xbe\n\tv_mov_b32 %3, v158"
                 : "=&v"(ma), "=&v"(mb), "=&v"(la), "=&v"(lb), "=&v"(bad) : "s"(tag), "s"(~tag) : "memory");
    if (__builtin_expect(__any(bad != 0), 0)) {
      _Pragma("nounroll") for (int sp = 0; sp < SPIN_MAX; ++sp) {
        const u32x4 m0 = ld<AUX_SC1>(rw, mo), m1 = ld<AUX_SC1>(rw, mo + 256);
        asm volatile("" ::: "memory");
        ma = U2F(m0[0]); mb = U2F(m1[0]); la = U2F(m0[2]); lb = U2F(m1[2]);
        if (!__any(((m0[1] ^ tag) | (m0[3] ^ ~tag) | (m1[1] ^ tag) | (m1[3] ^ ~tag)) != 0)) break;
      }
    }
    float M;
    asm volatile("v_max_f32 %0, %1, %2\n\ts_nop 1\n\tv_max_f32_dpp %0, %0, %0 row_ror:4 row_mask:0xf bank_mask:0xf\n\ts_nop 1\n\tv_max_f32_dpp %0, %0, %0 row_ror:8 row_mask:0xf bank_mask:0xf"
                 : "=&v"(M) : "v"(ma), "v"(mb));
    const float e0 = ex2(ma - M), e1 = ex2(mb - M);
    float Ls = e0 * la + e1 * lb; Ls += dpp<ROR4>(Ls); Ls += dpp<ROR8>(Ls);
    const float sc = gt * rcp(Ls);
    sts<f32x2>(sa, f32x2{e0 * sc, e1 * sc});
  }
  BAR();
  {
    const u32x4 a0 = lds<u32x4>(orr), a1 = lds<u32x4>(orr + 16 * OS), a2 = lds<u32x4>(orr + 64), a3 = lds<u32x4>(orr + 16 * OS + 64);
    const f32x2 sw = lds<f32x2>(sa);
    __builtin_amdgcn_sched_barrier(0);
    f32x4 d0 = mfma(wf0, a0, f32x4{0.f, 0.f, 0.f, 0.f}), d1 = mfma(wf0, a1, f32x4{0.f, 0.f, 0.f, 0.f});
    __builtin_amdgcn_sched_barrier(0);
    d0 = mfma(wf1, a2, d0); d1 = mfma(wf1, a3, d1);
    ab += sw[0] * d0; ab += sw[1] * d1;
  }
#pragma unroll
  for (int j = 0; j < 4; ++j) ab[j] += dpp<ROR4>(ab[j]);
#pragma unroll
  for (int j = 0; j < 4; ++j) ab[j] += dpp<ROR8>(ab[j]);
  st16(rw, oa, seal(ab, tb));
}

extern "C" void kernel_launch(void* const* d_in, const int* in_sizes, int n_in, void* d_out, int out_size, void* d_ws, size_t ws_size, hipStream_t stream) {
  fused_decode<<<256, 768, 0, stream>>>((const u16*)d_in[3], (const u16*)d_in[6], (const u16*)d_in[7], (const u16*)d_in[1], (const u16*)d_in[2], (const u16*)d_in[0],
      (unsigned char*)d_ws, (const u16*)d_in[9], (const u16*)d_in[4], (const u16*)d_in[8], (u16*)d_out);
}
```
